```python
import torch

def _softmax(x, axis=-1):
    return torch.softmax(x, dim=axis)
def _gelu(x):
    return torch.nn.functional.gelu(x, approximate="tanh")
def _silu(x):
    return torch.nn.functional.silu(x)
def _sigmoid(x):
    return torch.sigmoid(x)
def _layernorm(x, axis=-1, eps=1e-5):
    m = x.mean(dim=axis, keepdim=True)
    v = x.var(dim=axis, keepdim=True, unbiased=False)
    return (x - m) / torch.sqrt(v + eps)
def _scatter_add(tgt, idx, src, axis=0):
    return tgt.clone().index_add_(axis, idx, src)
def _segment_sum(src, seg, n):
    out = torch.zeros((n,) + src.shape[1:], dtype=src.dtype)
    return out.index_add_(0, seg, src)
def _pad(x, pw):
    flat = tuple(v for pair in reversed(pw) for v in pair)
    return torch.nn.functional.pad(x, flat)

kernel_name = 'concat_heads__gqa_block_cached__gqa_block_cached__decode'

def setup_inputs():

    _w = (lambda a: (lambda t: t.to(torch.bfloat16) if t.is_floating_point() else t)(torch.as_tensor(a)))
    def _A_setup():
        B, Sq, D = 1, 1, 576
        Hq, Hkv, Sk, Dh = 32, 16, 16384, 128
        assert Hq % Hkv == 0, (Hq, Hkv)
        return {
            "x": _w((lambda *s: torch.randn(*s, dtype=torch.bfloat16) * 0.8)(B, Sq, D)),
            "k_cache": _w((lambda *s: torch.randn(*s, dtype=torch.bfloat16) * 0.8)(B, Hkv, Sk, Dh)),
            "v_cache": _w((lambda *s: torch.randn(*s, dtype=torch.bfloat16) * 0.05)(B, Hkv, Sk, Dh)),
            "wq": _w((lambda *s: torch.randn(*s, dtype=torch.bfloat16) * 0.8)(D, Hq * Dh) * (D ** -0.5)),
            "wo": _w((lambda *s: torch.randn(*s, dtype=torch.bfloat16) * 0.05)(Hq * Dh, D) * ((Hq * Dh) ** -0.5)),
        }
    _A_in = _A_setup()

    def _B_setup():
        B, Sq, D = 1, 1, 576
        Hq, Hkv, Sk, Dh = 32, 16, 16384, 128
        assert Hq % Hkv == 0, (Hq, Hkv)
        return {
            "x": _w((lambda *s: torch.randn(*s, dtype=torch.bfloat16) * 0.8)(B, Sq, D)),
            "k_cache": _w((lambda *s: torch.randn(*s, dtype=torch.bfloat16) * 0.8)(B, Hkv, Sk, Dh)),
            "v_cache": _w((lambda *s: torch.randn(*s, dtype=torch.bfloat16) * 0.05)(B, Hkv, Sk, Dh)),
            "wq": _w((lambda *s: torch.randn(*s, dtype=torch.bfloat16) * 0.8)(D, Hq * Dh) * (D ** -0.5)),
            "wo": _w((lambda *s: torch.randn(*s, dtype=torch.bfloat16) * 0.05)(Hq * Dh, D) * ((Hq * Dh) ** -0.5)),
        }
    _B_in = _B_setup()

    return {
        "A_x": _A_in["x"],
        "A_k_cache": _A_in["k_cache"],
        "A_v_cache": _A_in["v_cache"],
        "A_wq": _A_in["wq"],
        "A_wo": _A_in["wo"],
        "B_x": _B_in["x"],
        "B_k_cache": _B_in["k_cache"],
        "B_v_cache": _B_in["v_cache"],
        "B_wq": _B_in["wq"],
        "B_wo": _B_in["wo"],
    }

def reference(A_x, A_k_cache, A_v_cache, A_wq, A_wo, B_x, B_k_cache, B_v_cache, B_wq, B_wo):
    def _A_ref(x, k_cache, v_cache, wq, wo):
        B, Sq, D = x.shape
        Hq, Dh = 32, 128
        Hkv = k_cache.shape[1]
        rep = Hq // Hkv
        q = (lambda x, *d: x.permute(*d))((x @ wq).reshape(B, Sq, Hq, Dh), 0, 2, 1, 3)
        k = (lambda x, n, axis: torch.repeat_interleave(x, n, dim=axis))(k_cache, rep, axis=1)
        v = (lambda x, n, axis: torch.repeat_interleave(x, n, dim=axis))(v_cache, rep, axis=1)
        scores = torch.einsum("bhqd,bhkd->bhqk", q, k) * (Dh ** -0.5)
        p = _softmax(scores, axis=-1)
        o = torch.einsum("bhqk,bhkd->bhqd", p, v)
        return (lambda x, *d: x.permute(*d))(o, 0, 2, 1, 3).reshape(B, Sq, Hq * Dh) @ wo

    def _B_ref(x, k_cache, v_cache, wq, wo):
        B, Sq, D = x.shape
        Hq, Dh = 32, 128
        Hkv = k_cache.shape[1]
        rep = Hq // Hkv
        q = (lambda x, *d: x.permute(*d))((x @ wq).reshape(B, Sq, Hq, Dh), 0, 2, 1, 3)
        k = (lambda x, n, axis: torch.repeat_interleave(x, n, dim=axis))(k_cache, rep, axis=1)
        v = (lambda x, n, axis: torch.repeat_interleave(x, n, dim=axis))(v_cache, rep, axis=1)
        scores = torch.einsum("bhqd,bhkd->bhqk", q, k) * (Dh ** -0.5)
        p = _softmax(scores, axis=-1)
        o = torch.einsum("bhqk,bhkd->bhqd", p, v)
        return (lambda x, *d: x.permute(*d))(o, 0, 2, 1, 3).reshape(B, Sq, Hq * Dh) @ wo

    a = _A_ref(A_x, A_k_cache, A_v_cache, A_wq, A_wo)
    b = _B_ref(B_x, B_k_cache, B_v_cache, B_wq, B_wo)
    return torch.concatenate([a, b], axis=-1)
```

```cpp
#include <hip/hip_runtime.h>
#include <hip/hip_bf16.h>
#include <stdint.h>

typedef unsigned u32x4 __attribute__((ext_vector_type(4)));
typedef float f32x4 __attribute__((ext_vector_type(4)));
typedef float f32x2 __attribute__((ext_vector_type(2)));
typedef __bf16 bf16x2 __attribute__((ext_vector_type(2)));

constexpr int DM = 576, HQ = 32, HKV = 16, SK = 16384, DH = 128, NQ = HQ * DH;
constexpr int NSPLIT = 8, NWAVE = 8;
constexpr int KEYS_PER_WG = SK / NSPLIT;
constexpr int TILE = 32;
constexpr int TILES_PER_WAVE = KEYS_PER_WG / (TILE * NWAVE);

__device__ __forceinline__ float dot2(unsigned k, unsigned q, float acc) {
  return __builtin_amdgcn_fdot2_f32_bf16(__builtin_bit_cast(bf16x2, k), __builtin_bit_cast(bf16x2, q), acc, false);
}
template <int CTRL> __device__ __forceinline__ float dpp(float x) {
  return __builtin_bit_cast(float, __builtin_amdgcn_mov_dpp(__builtin_bit_cast(int, x), CTRL, 0xf, 0xf, true));
}
__device__ __forceinline__ float bflo(unsigned u) { return __builtin_bit_cast(float, u << 16); }
__device__ __forceinline__ float bfhi(unsigned u) { return __builtin_bit_cast(float, u & 0xffff0000u); }
__device__ __forceinline__ unsigned short f2bf(float f) {
  __bf16 b = (__bf16)f; return __builtin_bit_cast(unsigned short, b);
}
constexpr int XOR1 = 0xB1, XOR2 = 0x4E, XOR7 = 0x141, XOR8 = 0x128, ROR4 = 0x124, ROR2 = 0x122;

__device__ __forceinline__ float xrow16_sum(float x) {
  auto s = __builtin_amdgcn_permlane16_swap(__float_as_uint(x), __float_as_uint(x), false, false);
  x = __uint_as_float(s[0]) + __uint_as_float(s[1]);
  auto t = __builtin_amdgcn_permlane32_swap(__float_as_uint(x), __float_as_uint(x), false, false);
  return __uint_as_float(t[0]) + __uint_as_float(t[1]);
}
__device__ __forceinline__ float xrow16_max(float x) {
  auto s = __builtin_amdgcn_permlane16_swap(__float_as_uint(x), __float_as_uint(x), false, false);
  x = fmaxf(__uint_as_float(s[0]), __uint_as_float(s[1]));
  auto t = __builtin_amdgcn_permlane32_swap(__float_as_uint(x), __float_as_uint(x), false, false);
  return fmaxf(__uint_as_float(t[0]), __uint_as_float(t[1]));
}

struct Ptrs {
  const unsigned short* x[2];
  const unsigned short* k[2];
  const unsigned short* v[2];
  const unsigned short* wq[2];
  const unsigned short* wo[2];
};

constexpr size_t WS_Q = 0, WS_PML = 16384, WS_PO = 32768, WS_OP = 32768 + 262144;

__global__ void __launch_bounds__(256) k_qproj(Ptrs p, unsigned char* ws) {
  __shared__ float red[32][64];
  const int br = blockIdx.x >> 6, cc = blockIdx.x & 63;
  const int tid = threadIdx.x, cg = tid & 7, rg = tid >> 3;
  const unsigned short* x = p.x[br];
  const unsigned short* wq = p.wq[br] + cc * 64 + cg * 8;
  float acc[8];
  for (int i = 0; i < 8; ++i) acc[i] = 0.f;
#pragma unroll
  for (int i = 0; i < 18; ++i) {
    const int row = rg + 32 * i;
    const u32x4 w = __builtin_nontemporal_load(reinterpret_cast<const u32x4*>(wq + (size_t)row * NQ));
    const float xv = bflo(x[row]);
#pragma unroll
    for (int k = 0; k < 4; ++k) { acc[2 * k] = fmaf(xv, bflo(w[k]), acc[2 * k]); acc[2 * k + 1] = fmaf(xv, bfhi(w[k]), acc[2 * k + 1]); }
  }
  for (int i = 0; i < 8; ++i) red[rg][cg * 8 + i] = acc[i];
  __syncthreads();
  if (tid < 64) {
    float s = 0.f;
    for (int r = 0; r < 32; ++r) s += red[r][tid];
    reinterpret_cast<unsigned short*>(ws + WS_Q)[br * NQ + cc * 64 + tid] = f2bf(s);
  }
}

__global__ void __launch_bounds__(512, 2) k_attn(Ptrs p, unsigned char* ws) {
  __shared__ __attribute__((aligned(16))) float mrg[NWAVE][2][4 + DH];
  const int bid = blockIdx.x;
  const int split = bid & 7, grp = bid >> 3, br = grp >> 4, kvh = grp & 15;
  const int tid = threadIdx.x, lane = tid & 63, wave = __builtin_amdgcn_readfirstlane(tid >> 6), r = lane >> 4, c = lane & 15;
  constexpr float C2 = 0.088388347648318440f * 1.4426950408889634f;
  const unsigned short* qp = reinterpret_cast<const unsigned short*>(ws + WS_Q) + br * NQ + kvh * 2 * DH + 8 * c;
  const u32x4 q0 = *reinterpret_cast<const u32x4*>(qp);
  const u32x4 q1 = *reinterpret_cast<const u32x4*>(qp + DH);
  const size_t base = ((size_t)kvh * SK + (size_t)split * KEYS_PER_WG + wave * TILE + r) * DH + 8 * c;
  const unsigned short* kb = p.k[br] + base;
  const unsigned short* vb = p.v[br] + base;
  u32x4 Kn[8], Vn[8];
  float m = -INFINITY, l = 0.f, acc[2][8];
  for (int i = 0; i < 8; ++i) { acc[0][i] = 0.f; acc[1][i] = 0.f; }
#define ISSUE(it) do { _Pragma("unroll") for (int t = 0; t < 8; ++t) Kn[t] = __builtin_nontemporal_load(reinterpret_cast<const u32x4*>(kb + (size_t)(it) * (NWAVE * TILE * DH) + t * 4 * DH)); \
    _Pragma("unroll") for (int t = 0; t < 8; ++t) Vn[t] = __builtin_nontemporal_load(reinterpret_cast<const u32x4*>(vb + (size_t)(it) * (NWAVE * TILE * DH) + t * 4 * DH)); } while (0)
  ISSUE(0);
  for (int it = 0; it < TILES_PER_WAVE; ++it) {
    u32x4 Kc[8], Vc[8];
#pragma unroll
    for (int t = 0; t < 8; ++t) { Kc[t] = Kn[t]; Vc[t] = Vn[t]; }
    if (it + 1 < TILES_PER_WAVE) ISSUE(it + 1);
    float x[16];
#pragma unroll
    for (int t = 0; t < 8; ++t) {
      float d0 = 0.f, d1 = 0.f;
#pragma unroll
      for (int k = 0; k < 4; ++k) { d0 = dot2(Kc[t][k], q0[k], d0); d1 = dot2(Kc[t][k], q1[k], d1); }
      x[2 * t] = d0; x[2 * t + 1] = d1;
    }
#define TR_STEP(HALF, CTRL, BIT) _Pragma("unroll") for (int h = 0; h < (HALF); ++h) { \
      const float lo_ = x[h], hi_ = x[h + (HALF)]; const float t1_ = lo_ + dpp<CTRL>(lo_), t2_ = hi_ + dpp<CTRL>(hi_); \
      x[h] = (c & (BIT)) ? t2_ : t1_; }
    TR_STEP(8, XOR8, 8) TR_STEP(4, XOR7, 4) TR_STEP(2, XOR2, 2) TR_STEP(1, XOR1, 1)
#undef TR_STEP
    const float sc = x[0] * C2;
    float pm = sc;
    pm = fmaxf(pm, dpp<XOR2>(pm)); pm = fmaxf(pm, dpp<ROR4>(pm)); pm = fmaxf(pm, dpp<XOR8>(pm));
    const float mn = fmaxf(m, pm);
    const float alpha = __builtin_amdgcn_exp2f(m - mn);
    const float pe = __builtin_amdgcn_exp2f(sc - mn);
    l = fmaf(l, alpha, pe);
    m = mn;
    const float a0 = dpp<0x150>(alpha), a1 = dpp<0x151>(alpha);
#pragma unroll
    for (int k = 0; k < 8; ++k) { acc[0][k] *= a0; acc[1][k] *= a1; }
#define PV(t) { const float p0 = dpp<0x150 + 2 * t>(pe), p1 = dpp<0x151 + 2 * t>(pe); \
      _Pragma("unroll") for (int k = 0; k < 4; ++k) { const float lo = bflo(Vc[t][k]), hi = bfhi(Vc[t][k]); \
        acc[0][2 * k] = fmaf(p0, lo, acc[0][2 * k]); acc[0][2 * k + 1] = fmaf(p0, hi, acc[0][2 * k + 1]); \
        acc[1][2 * k] = fmaf(p1, lo, acc[1][2 * k]); acc[1][2 * k + 1] = fmaf(p1, hi, acc[1][2 * k + 1]); } }
    PV(0) PV(1) PV(2) PV(3) PV(4) PV(5) PV(6) PV(7)
#undef PV
  }
  l += dpp<XOR2>(l); l += dpp<ROR4>(l); l += dpp<XOR8>(l);
  const float M = xrow16_max(m);
  const float w = __builtin_amdgcn_exp2f(m - M);
  const float L = xrow16_sum(l * w);
  const float w0 = dpp<0x150>(w), w1 = dpp<0x151>(w);
#pragma unroll
  for (int k = 0; k < 8; ++k) { acc[0][k] = xrow16_sum(acc[0][k] * w0); acc[1][k] = xrow16_sum(acc[1][k] * w1); }
  if (r == 0) {
#pragma unroll
    for (int h = 0; h < 2; ++h) {
      *reinterpret_cast<f32x4*>(&mrg[wave][h][4 + 8 * c]) = f32x4{acc[h][0], acc[h][1], acc[h][2], acc[h][3]};
      *reinterpret_cast<f32x4*>(&mrg[wave][h][4 + 8 * c + 4]) = f32x4{acc[h][4], acc[h][5], acc[h][6], acc[h][7]};
    }
    if (c < 2) { mrg[wave][c][0] = M; mrg[wave][c][1] = L; }
  }
  __syncthreads();
  if (tid < 256) {
    const int h = tid >> 7, d = tid & 127;
    float MM = -INFINITY;
    for (int w8 = 0; w8 < NWAVE; ++w8) MM = fmaxf(MM, mrg[w8][h][0]);
    float LL = 0.f, o = 0.f;
    for (int w8 = 0; w8 < NWAVE; ++w8) { const float ww = __builtin_amdgcn_exp2f(mrg[w8][h][0] - MM); LL = fmaf(ww, mrg[w8][h][1], LL); o = fmaf(ww, mrg[w8][h][4 + d], o); }
    const int head = br * HQ + kvh * 2 + h;
    reinterpret_cast<float*>(ws + WS_PO)[(head * NSPLIT + split) * DH + d] = o;
    if (d == 0) { float* q = reinterpret_cast<float*>(ws + WS_PML) + (head * NSPLIT + split) * 2; q[0] = MM; q[1] = LL; }
  }
}

__global__ void __launch_bounds__(256) k_oproj(Ptrs p, unsigned char* ws) {
  __shared__ float o_s[512];
  __shared__ float red[32][64];
  const int cc = blockIdx.x % 9, rc = (blockIdx.x / 9) & 7, br = blockIdx.x / 72;
  const int tid = threadIdx.x;
  for (int i = tid; i < 512; i += 256) {
    const int head = br * HQ + rc * 4 + (i >> 7), d = i & 127;
    const float* pml = reinterpret_cast<const float*>(ws + WS_PML) + head * NSPLIT * 2;
    const float* po = reinterpret_cast<const float*>(ws + WS_PO) + head * NSPLIT * DH + d;
    float M = -INFINITY;
    for (int s = 0; s < NSPLIT; ++s) M = fmaxf(M, pml[2 * s]);
    float L = 0.f, o = 0.f;
    for (int s = 0; s < NSPLIT; ++s) { const float w = __builtin_amdgcn_exp2f(pml[2 * s] - M); L = fmaf(w, pml[2 * s + 1], L); o = fmaf(w, po[s * DH], o); }
    o_s[i] = bflo(f2bf(o / L));
  }
  __syncthreads();
  const int cg = tid & 7, rg = tid >> 3;
  const unsigned short* wo = p.wo[br] + (size_t)(rc * 512) * DM + cc * 64 + cg * 8;
  float acc[8];
  for (int i = 0; i < 8; ++i) acc[i] = 0.f;
#pragma unroll
  for (int i = 0; i < 16; ++i) {
    const int row = rg + 32 * i;
    const u32x4 w = __builtin_nontemporal_load(reinterpret_cast<const u32x4*>(wo + (size_t)row * DM));
    const float xv = o_s[row];
#pragma unroll
    for (int k = 0; k < 4; ++k) { acc[2 * k] = fmaf(xv, bflo(w[k]), acc[2 * k]); acc[2 * k + 1] = fmaf(xv, bfhi(w[k]), acc[2 * k + 1]); }
  }
  for (int i = 0; i < 8; ++i) red[rg][cg * 8 + i] = acc[i];
  __syncthreads();
  if (tid < 64) {
    float s = 0.f;
    for (int r = 0; r < 32; ++r) s += red[r][tid];
    reinterpret_cast<float*>(ws + WS_OP)[(br * 8 + rc) * DM + cc * 64 + tid] = s;
  }
}

__global__ void __launch_bounds__(256) k_final(const unsigned char* ws, unsigned short* out) {
  const int i = blockIdx.x * 256 + threadIdx.x;
  if (i < 2 * DM) {
    const int br = i / DM, col = i % DM;
    float s = 0.f;
    for (int rc = 0; rc < 8; ++rc) s += reinterpret_cast<const float*>(ws + WS_OP)[(br * 8 + rc) * DM + col];
    out[i] = f2bf(s);
  }
}

extern "C" void kernel_launch(void* const* d_in, const int* in_sizes, int n_in, void* d_out, int out_size, void* d_ws, size_t ws_size, hipStream_t stream) {
  Ptrs p;
  for (int b = 0; b < 2; ++b) {
    p.x[b] = (const unsigned short*)d_in[5 * b + 0];
    p.k[b] = (const unsigned short*)d_in[5 * b + 1];
    p.v[b] = (const unsigned short*)d_in[5 * b + 2];
    p.wq[b] = (const unsigned short*)d_in[5 * b + 3];
    p.wo[b] = (const unsigned short*)d_in[5 * b + 4];
  }
  unsigned char* ws = (unsigned char*)d_ws;
  k_qproj<<<128, 256, 0, stream>>>(p, ws);
  k_attn<<<256, 512, 0, stream>>>(p, ws);
  k_oproj<<<144, 256, 0, stream>>>(p, ws);
  k_final<<<5, 256, 0, stream>>>(ws, (unsigned short*)d_out);
}
```

```cpp
#define DYN 0
#include <hip/hip_runtime.h>
#include <stdint.h>

typedef unsigned u32x4 __attribute__((ext_vector_type(4)));
typedef unsigned u32x2 __attribute__((ext_vector_type(2)));
typedef float f32x4 __attribute__((ext_vector_type(4)));
typedef float f32x2 __attribute__((ext_vector_type(2)));
typedef __bf16 bf16x2 __attribute__((ext_vector_type(2)));
#define LAS __attribute__((address_space(3)))

constexpr int DM = 576, NQ = 4096;
constexpr int NSLOT = 18, SLOTB = 8192;
#define INFL 3
constexpr int TD = 4;
constexpr int NS = DYN ? NSLOT + TD : 128;
constexpr int NT = 1024;
constexpr int NC = 8;
constexpr unsigned FINAL = 0x40000000u;
constexpr int L_CTRL = NSLOT * SLOTB;
constexpr int L_QPART = L_CTRL + 128;
constexpr int L_QBUF = L_QPART + 2048;
constexpr int L_MRG = L_QBUF + 512;
constexpr int L_OBUF = L_MRG + NC * 2 * 528;
constexpr int L_TOTAL = L_OBUF + 128;
constexpr int W_EPOCH = 0;
constexpr int W_CNT = 4096;
constexpr int W_LSE = 16384;
constexpr int W_Q = 32768;
constexpr int W_P = W_Q + 32 * 4096;
constexpr int W_O = W_P + 32 * 16384;
constexpr int W_END = W_O + 2 * 128 * 4608;
constexpr int SPIN_CAP = 1 << 19;
constexpr int GSPIN_CAP = 1 << 16;
#define STAMP(k) do {} while (0)

struct Params {
  const unsigned short* in[10];
  unsigned short* out;
  unsigned char* ws;
};

__device__ __forceinline__ float dot2(unsigned k, unsigned q, float acc) {
  return __builtin_amdgcn_fdot2_f32_bf16(__builtin_bit_cast(bf16x2, k), __builtin_bit_cast(bf16x2, q), acc, false);
}
template <int CTRL> __device__ __forceinline__ float dpp(float x) {
  return __builtin_bit_cast(float, __builtin_amdgcn_mov_dpp(__builtin_bit_cast(int, x), CTRL, 0xf, 0xf, true));
}
__device__ __forceinline__ float bflo(unsigned u) { return __builtin_bit_cast(float, u << 16); }
__device__ __forceinline__ float bfhi(unsigned u) { return __builtin_bit_cast(float, u & 0xffff0000u); }
__device__ __forceinline__ unsigned pack_bf16(float lo, float hi) {
  bf16x2 v = {(__bf16)lo, (__bf16)hi};
  return __builtin_bit_cast(unsigned, v);
}
__device__ __forceinline__ float fu(unsigned u) { return __builtin_bit_cast(float, u); }
__device__ __forceinline__ unsigned uf(float f) { return __builtin_bit_cast(unsigned, f); }
constexpr int XOR1 = 0xB1, XOR2 = 0x4E, XOR7 = 0x141, XOR8 = 0x128, ROR4 = 0x124, BCAST = 0x150;
__device__ __forceinline__ float swap32_add(float a, float b) {
  auto s = __builtin_amdgcn_permlane32_swap(uf(a), uf(b), false, false);
  return fu(s[0]) + fu(s[1]);
}
__device__ __forceinline__ float swap16_add(float a, float b) {
  auto s = __builtin_amdgcn_permlane16_swap(uf(a), uf(b), false, false);
  return fu(s[0]) + fu(s[1]);
}
__device__ __forceinline__ float xrow_max(float x) {
  auto s = __builtin_amdgcn_permlane16_swap(uf(x), uf(x), false, false);
  x = fmaxf(fu(s[0]), fu(s[1]));
  auto t = __builtin_amdgcn_permlane32_swap(uf(x), uf(x), false, false);
  return fmaxf(fu(t[0]), fu(t[1]));
}
#define BARRIER() asm volatile("s_waitcnt lgkmcnt(0)\n\ts_barrier" ::: "memory")
constexpr int AUX_NT = 2, AUX_SC1 = 16;

__global__ void __launch_bounds__(576) attn_fused(Params p) {
  extern __shared__ __attribute__((aligned(16))) unsigned char smem[];
  LAS unsigned char* L = (LAS unsigned char*)smem;
  volatile LAS unsigned* ctrl = (volatile LAS unsigned*)(L + L_CTRL);
  const int tid = threadIdx.x, lane = tid & 63, wave = __builtin_amdgcn_readfirstlane(tid >> 6);
  const int bid = blockIdx.x, j = bid & 7, g = bid >> 3, br = g >> 4, kvh = g & 15;
  const __amdgpu_buffer_rsrc_t rws = __builtin_amdgcn_make_buffer_rsrc((void*)p.ws, 0, W_END, 0x00020000);
  const __amdgpu_buffer_rsrc_t rwo = __builtin_amdgcn_make_buffer_rsrc((void*)(p.in[5 * br + 4] + (size_t)(kvh * 256 + 32 * j) * DM), 0, 32 * DM * 2, 0x00020000);
  u32x4 wo[4];
  const int wo_off = (lane >> 3) * (4 * DM * 2) + (8 * wave + (lane & 7)) * 16;
  if (wave == 0) STAMP(0);

  if (wave == 0) {
    const __amdgpu_buffer_rsrc_t rk = __builtin_amdgcn_make_buffer_rsrc((void*)(p.in[5 * br + 1] + (size_t)kvh * (16384 * 128)), 0, 4 << 20, 0x00020000);
    const __amdgpu_buffer_rsrc_t rv = __builtin_amdgcn_make_buffer_rsrc((void*)(p.in[5 * br + 2] + (size_t)kvh * (16384 * 128)), 0, 4 << 20, 0x00020000);
    const unsigned ldsbase = (unsigned)(uintptr_t)L;
    const unsigned voff = lane * 16;
    const unsigned vctrl = ldsbase + L_CTRL;
    const unsigned vinit = ldsbase + L_CTRL + (lane & 31) * 4;
    unsigned* cnt = (unsigned*)(p.ws + W_CNT + g * 256);
    unsigned vt, vtmp, vzero, vone;
    unsigned si, sslot, ssoff, st0, st1, sspin;
    asm volatile(
      "v_mov_b32 %[vzero], 0\n\t"
      "v_mov_b32 %[vone], 1\n\t"
      "v_mov_b32 %[vt], -1\n\t"
      "ds_write_b32 %[vinit], %[vzero]\n\t"
      "s_mov_b32 %[si], 0\n\t"
      "s_mov_b32 %[sslot], %[ldsbase]\n\t"
      ".Lloop%=:\n\t"
      "s_lshl_b32 %[st0], %[si], 3\n\t"
      "s_add_u32 %[st0], %[st0], %[j]\n\t"
      "s_cmp_lt_u32 %[si], %[NSc]\n\t"
      "s_cbranch_scc1 .Lhave%=\n\t"
      "s_branch .Ldrain%=\n\t"
      "s_and_b32 %[st1], %[si], 7\n\t"
      "v_readlane_b32 %[st0], %[vt], %[st1]\n\t"
      "s_cmp_lg_u32 %[st0], -1\n\t"
      "s_cbranch_scc1 .Lok%=\n\t"
      "s_waitcnt vmcnt(0)\n\t"
      "v_readlane_b32 %[st0], %[vt], %[st1]\n\t"
      ".Lok%=:\n\t"
      "s_add_u32 %[st0], %[st0], %[NS8]\n\t"
      "s_cmp_ge_u32 %[st0], %[NTc]\n\t"
      "s_cbranch_scc1 .Ldrain%=\n\t"
      ".Lhave%=:\n\t"
      "s_lshl_b32 %[ssoff], %[st0], 12\n\t"
      "s_cmp_lt_u32 %[si], %[NSLOTc]\n\t"
      "s_cbranch_scc1 .Lissue%=\n\t"
      "s_sub_u32 %[st0], %[si], %[NSLOTc]\n\t"
      "s_and_b32 %[st1], %[st0], 7\n\t"
      "s_lshl_b32 %[st1], %[st1], 2\n\t"
      "v_add_u32 %[vtmp], %[st1], %[vctrl]\n\t"
      "s_lshr_b32 %[st0], %[st0], 3\n\t"
      "ds_read_b32 %[vzero], %[vtmp] offset:32\n\t"
      "s_waitcnt lgkmcnt(0)\n\t"
      "v_readfirstlane_b32 %[st1], %[vzero]\n\t"
      "s_cmp_gt_u32 %[st1], %[st0]\n\t"
      "s_cbranch_scc1 .Lfree%=\n\t"
      "s_waitcnt vmcnt(0)\n\t"
      "v_mov_b32 %[vzero], %[si]\n\t"
      "s_mov_b64 exec, 1\n\t"
      "ds_write_b32 %[vctrl], %[vzero]\n\t"
      "s_mov_b64 exec, -1\n\t"
      "s_mov_b32 %[sspin], 0\n\t"
      ".Lspin%=:\n\t"
      "s_sleep 1\n\t"
      "ds_read_b32 %[vzero], %[vtmp] offset:32\n\t"
      "s_add_u32 %[sspin], %[sspin], 1\n\t"
      "s_cmp_gt_u32 %[sspin], %[CAP]\n\t"
      "s_cbranch_scc1 .Ldrain%=\n\t"
      "s_waitcnt lgkmcnt(0)\n\t"
      "v_readfirstlane_b32 %[st1], %[vzero]\n\t"
      "s_cmp_gt_u32 %[st1], %[st0]\n\t"
      "s_cbranch_scc0 .Lspin%=\n\t"
      ".Lfree%=:\n\t"
      "s_branch .Lissue%=\n\t"
      "s_add_u32 %[st0], %[si], %[TDc]\n\t"
      "s_and_b32 %[st0], %[st0], 7\n\t"
      "v_mov_b32 %[vzero], 0\n\t"
      "v_writelane_b32 %[vt], -1, %[st0]\n\t"
      "s_lshl_b64 exec, 1, %[st0]\n\t"
      "global_atomic_add %[vt], %[vzero], %[vone], %[cnt] sc0\n\t"
      "s_mov_b64 exec, -1\n\t"
      ".Lissue%=:\n\t"
      "s_mov_b32 m0, %[sslot]\n\t"
      "s_add_u32 %[st0], %[sslot], 4096\n\t"
      "buffer_load_dwordx4 %[voff], %[rk], %[ssoff] offen nt lds\n\t"
      "buffer_load_dwordx4 %[voff], %[rk], %[ssoff] offen offset:1024 nt lds\n\t"
      "buffer_load_dwordx4 %[voff], %[rk], %[ssoff] offen offset:2048 nt lds\n\t"
      "buffer_load_dwordx4 %[voff], %[rk], %[ssoff] offen offset:3072 nt lds\n\t"
      "s_mov_b32 m0, %[st0]\n\t"
      "s_add_u32 %[sslot], %[sslot], 8192\n\t"
      "buffer_load_dwordx4 %[voff], %[rv], %[ssoff] offen nt lds\n\t"
      "buffer_load_dwordx4 %[voff], %[rv], %[ssoff] offen offset:1024 nt lds\n\t"
      "buffer_load_dwordx4 %[voff], %[rv], %[ssoff] offen offset:2048 nt lds\n\t"
      "buffer_load_dwordx4 %[voff], %[rv], %[ssoff] offen offset:3072 nt lds\n\t"
      "s_cmp_lt_u32 %[sslot], %[ldsend]\n\t"
      "s_cselect_b32 %[sslot], %[sslot], %[ldsbase]\n\t"
      "s_add_u32 %[si], %[si], 1\n\t"
      "s_cmp_lg_u32 %[si], %[INFL1]\n\t"
      "s_cbranch_scc1 .Lnobar%=\n\t"
      "s_waitcnt lgkmcnt(0)\n\t"
      "s_barrier\n\t"
      ".Lnobar%=:\n\t"
      "s_cmp_le_u32 %[si], %[INFLc]\n\t"
      "s_cbranch_scc1 .Lloop%=\n\t"
      "s_sub_u32 %[st0], %[si], %[INFLc]\n\t"
      "v_mov_b32 %[vtmp], %[st0]\n\t"
      "s_mov_b64 exec, 1\n\t"
      "s_waitcnt vmcnt(%[W])\n\t"
      "ds_max_u32 %[vctrl], %[vtmp]\n\t"
      "s_mov_b64 exec, -1\n\t"
      "s_branch .Lloop%=\n\t"
      ".Ldrain%=:\n\t"
      "s_mov_b64 exec, 1\n\t"
      "s_sub_u32 %[st0], %[si], 2\n\t"
      "v_mov_b32 %[vtmp], %[st0]\n\t"
      "s_waitcnt vmcnt(16)\n\t"
      "ds_max_u32 %[vctrl], %[vtmp]\n\t"
      "s_sub_u32 %[st0], %[si], 1\n\t"
      "v_mov_b32 %[vzero], %[st0]\n\t"
      "s_waitcnt vmcnt(8)\n\t"
      "ds_max_u32 %[vctrl], %[vzero]\n\t"
      "s_or_b32 %[st0], %[si], %[FIN]\n\t"
      "v_mov_b32 %[vone], %[st0]\n\t"
      "s_waitcnt vmcnt(0)\n\t"
      "ds_write_b32 %[vctrl], %[vone]\n\t"
      "s_mov_b64 exec, -1\n\t"
      "s_waitcnt lgkmcnt(0)\n\t"
      : [vt] "=&v"(vt), [vtmp] "=&v"(vtmp), [vzero] "=&v"(vzero), [vone] "=&v"(vone),
        [si] "=&s"(si), [sslot] "=&s"(sslot), [ssoff] "=&s"(ssoff), [st0] "=&s"(st0), [st1] "=&s"(st1), [sspin] "=&s"(sspin)
      : [voff] "v"(voff), [vctrl] "v"(vctrl), [vinit] "v"(vinit), [rk] "s"(rk), [rv] "s"(rv), [cnt] "s"(cnt), [j] "s"(j),
        [ldsbase] "s"(ldsbase), [ldsend] "s"(ldsbase + NSLOT * SLOTB),
        [NSc] "n"(NS), [NS8] "n"(NS * 8), [NTc] "n"(NT), [NSLOTc] "n"(NSLOT), [TDc] "n"(TD), [INFLc] "n"(INFL), [INFL1] "n"(INFL + 1), [W] "n"(INFL * 8),
        [FIN] "n"(FINAL), [CAP] "n"(SPIN_CAP)
      : "memory", "scc", "m0");
    STAMP(4);
#pragma unroll
    for (int i = 0; i < 4; ++i) wo[i] = __builtin_amdgcn_raw_buffer_load_b128(rwo, wo_off + i * (DM * 2), 0, AUX_NT);
  } else {
    const int wi = wave - 1;
    const int rh = j >> 2, cc = j & 3;
    unsigned tag = 0;
    if (wi == 7) {
      tag = __builtin_amdgcn_raw_buffer_load_b32(rws, W_EPOCH + br * 256, 0, AUX_SC1);
      if (j == 0 && lane == 0) __builtin_amdgcn_raw_buffer_store_b32(0u, rws, W_CNT + g * 256, 0, AUX_SC1);
    }
    {
      const __amdgpu_buffer_rsrc_t rq = __builtin_amdgcn_make_buffer_rsrc((void*)(p.in[5 * br + 3] + (size_t)(288 * rh) * NQ + kvh * 256 + 64 * cc), 0, 288 * NQ * 2 - (kvh * 256 + 64 * cc) * 2, 0x00020000);
      const __amdgpu_buffer_rsrc_t rx = __builtin_amdgcn_make_buffer_rsrc((void*)(p.in[5 * br] + 288 * rh), 0, 288 * 2, 0x00020000);
      const int row0 = 8 * wi + (lane >> 3);
      u32x4 w[5];
      unsigned short xv[5];
#pragma unroll
      for (int k = 0; k < 5; ++k) {
        xv[k] = __builtin_amdgcn_raw_buffer_load_b16(rx, (row0 + 64 * k) * 2, 0, 0);
        w[k] = __builtin_amdgcn_raw_buffer_load_b128(rq, (row0 + 64 * k) * (NQ * 2) + (lane & 7) * 16, 0, AUX_NT);
      }
      __builtin_amdgcn_sched_barrier(0);
      asm volatile("s_barrier" ::: "memory");
      __builtin_amdgcn_sched_barrier(0);
      float a[8];
#pragma unroll
      for (int e = 0; e < 8; ++e) a[e] = 0.f;
#pragma unroll
      for (int k = 0; k < 5; ++k) {
        const float xf = bflo(xv[k]);
#pragma unroll
        for (int e = 0; e < 4; ++e) { a[2 * e] = fmaf(xf, bflo(w[k][e]), a[2 * e]); a[2 * e + 1] = fmaf(xf, bfhi(w[k][e]), a[2 * e + 1]); }
      }
      float u4[4], u2[2];
#pragma unroll
      for (int e = 0; e < 4; ++e) u4[e] = swap32_add(a[e], a[e + 4]);
#pragma unroll
      for (int e = 0; e < 2; ++e) u2[e] = swap16_add(u4[e], u4[e + 2]);
      const float t0 = u2[0] + dpp<XOR8>(u2[0]), t1 = u2[1] + dpp<XOR8>(u2[1]);
      const float r = (lane & 8) ? t1 : t0;
      ((LAS float*)(L + L_QPART))[wi * 64 + (lane & 7) * 8 + (lane >> 3)] = r;
      asm volatile("s_waitcnt lgkmcnt(0)" ::: "memory");
      if (lane == 0) __hip_atomic_fetch_add((LAS unsigned*)(L + L_CTRL) + 1, 1u, __ATOMIC_RELAXED, __HIP_MEMORY_SCOPE_WORKGROUP);
    }
    if (wi == 7) {
      STAMP(1);
      tag = __builtin_amdgcn_readfirstlane(tag) + 1u;
      for (int sp = 0; ctrl[1] < 8u && sp < SPIN_CAP; ++sp) {}
      asm volatile("" ::: "memory");
      f32x2 s2 = {0.f, 0.f};
#pragma unroll
      for (int w8 = 0; w8 < 8; ++w8) s2 += *(LAS f32x2*)(L + L_QPART + w8 * 256 + (lane & 31) * 8);
      asm volatile("s_waitcnt vmcnt(0)" ::: "memory");
      if (lane < 32) __builtin_amdgcn_raw_buffer_store_b128(u32x4{uf(s2[0]), tag, uf(s2[1]), tag}, rws, W_Q + g * 4096 + j * 512 + lane * 16, 0, AUX_SC1);
      u32x4 v[4];
#pragma nounroll
      for (int sp = 0; sp < GSPIN_CAP; ++sp) {
        bool ok = true;
#pragma unroll
        for (int k = 0; k < 4; ++k) v[k] = __builtin_amdgcn_raw_buffer_load_b128(rws, W_Q + g * 4096 + k * 1024 + lane * 16, 0, AUX_SC1);
#pragma unroll
        for (int k = 0; k < 4; ++k) ok &= (v[k][1] == tag) & (v[k][3] == tag);
        if (__all(ok)) break;
        asm volatile("" ::: "memory");
      }
#pragma unroll
      for (int k = 0; k < 2; ++k)
        ((LAS unsigned*)(L + L_QBUF))[k * 64 + lane] = pack_bf16(fu(v[k][0]) + fu(v[k + 2][0]), fu(v[k][2]) + fu(v[k + 2][2]));
      if (lane == 0) ctrl[3] = tag;
      asm volatile("s_waitcnt lgkmcnt(0)" ::: "memory");
      if (lane == 0) ctrl[2] = 1u;
      STAMP(2);
    }
    for (int sp = 0; ctrl[2] == 0u && sp < SPIN_CAP; ++sp) __builtin_amdgcn_s_sleep(1);
    asm volatile("" ::: "memory");
    const int r = lane >> 4, c = lane & 15;
    const u32x4 q0 = *(LAS u32x4*)(L + L_QBUF + c * 16), q1 = *(LAS u32x4*)(L + L_QBUF + 256 + c * 16);
#pragma unroll
    for (int i = 0; i < 4; ++i) wo[i] = __builtin_amdgcn_raw_buffer_load_b128(rwo, wo_off + i * (DM * 2), 0, AUX_NT);
    constexpr float C2 = 0.088388347648318440f * 1.4426950408889634f;
    float m = -INFINITY, l = 0.f, acc[2][8];
#pragma unroll
    for (int e = 0; e < 8; ++e) { acc[0][e] = 0.f; acc[1][e] = 0.f; }
    int slot = wi;
    unsigned done = 0;
    for (unsigned f = wi;; f += NC) {
      unsigned lv;
#pragma nounroll
      for (int sp = 0; sp < SPIN_CAP; ++sp) {
        lv = __builtin_amdgcn_readfirstlane(ctrl[0]);
        if (lv > f) break;
        __builtin_amdgcn_s_sleep(1);
      }
      if ((lv & (FINAL - 1)) <= f) break;
      asm volatile("" ::: "memory");
      const LAS u32x4* sp4 = (const LAS u32x4*)(L + slot * SLOTB) + lane;
      u32x4 K[4], V[4];
#pragma unroll
      for (int t = 0; t < 4; ++t) K[t] = sp4[t * 64];
#pragma unroll
      for (int t = 0; t < 4; ++t) V[t] = sp4[256 + t * 64];
      asm volatile("s_waitcnt lgkmcnt(0)" ::: "memory");
      ++done;
      if (lane == 0) ctrl[8 + wi] = done;
      asm volatile("" : "+v"(K[0]), "+v"(K[1]), "+v"(K[2]), "+v"(K[3]), "+v"(V[0]), "+v"(V[1]), "+v"(V[2]), "+v"(V[3])::"memory");
      slot += NC; if (slot >= NSLOT) slot -= NSLOT;
      float x[8];
#pragma unroll
      for (int t = 0; t < 4; ++t) {
        float d0 = 0.f, d1 = 0.f;
#pragma unroll
        for (int k = 0; k < 4; ++k) { d0 = dot2(K[t][k], q0[k], d0); d1 = dot2(K[t][k], q1[k], d1); }
        x[2 * t] = d0; x[2 * t + 1] = d1;
      }
#define TR_STEP(HALF, CTRL, BIT) _Pragma("unroll") for (int h = 0; h < (HALF); ++h) { \
        const float lo_ = x[h], hi_ = x[h + (HALF)]; const float t1_ = lo_ + dpp<CTRL>(lo_), t2_ = hi_ + dpp<CTRL>(hi_); \
        x[h] = (c & (BIT)) ? t2_ : t1_; }
      TR_STEP(4, XOR8, 8) TR_STEP(2, XOR7, 4) TR_STEP(1, XOR2, 2)
#undef TR_STEP
      const float sc = (x[0] + dpp<XOR1>(x[0])) * C2;
      float pm = fmaxf(sc, dpp<ROR4>(sc)); pm = fmaxf(pm, dpp<XOR8>(pm));
      const float mn = fmaxf(m, pm);
      const float alpha = __builtin_amdgcn_exp2f(m - mn);
      const float pe = __builtin_amdgcn_exp2f(sc - mn);
      l = fmaf(l, alpha, pe);
      m = mn;
      const float a0 = dpp<BCAST + 0>(alpha), a1 = dpp<BCAST + 2>(alpha);
#pragma unroll
      for (int e = 0; e < 8; ++e) { acc[0][e] *= a0; acc[1][e] *= a1; }
#define PV(t) { const float p0 = dpp<BCAST + 4 * t>(pe), p1 = dpp<BCAST + 4 * t + 2>(pe); \
        _Pragma("unroll") for (int k = 0; k < 4; ++k) { const float lo = bflo(V[t][k]), hi = bfhi(V[t][k]); \
          acc[0][2 * k] = fmaf(p0, lo, acc[0][2 * k]); acc[0][2 * k + 1] = fmaf(p0, hi, acc[0][2 * k + 1]); \
          acc[1][2 * k] = fmaf(p1, lo, acc[1][2 * k]); acc[1][2 * k + 1] = fmaf(p1, hi, acc[1][2 * k + 1]); } }
      PV(0) PV(1) PV(2) PV(3)
#undef PV
    }
    l += dpp<ROR4>(l); l += dpp<XOR8>(l);
    const float M = xrow_max(m);
    const float w = __builtin_amdgcn_exp2f(m - M);
    float Ls = l * w;
    Ls = swap16_add(Ls, Ls); Ls = swap32_add(Ls, Ls);
    const float w0 = dpp<BCAST + 0>(w), w1 = dpp<BCAST + 2>(w);
    float u[8];
#pragma unroll
    for (int e = 0; e < 8; ++e) u[e] = swap32_add(acc[0][e] * w0, acc[1][e] * w1);
    f32x4 z;
#pragma unroll
    for (int e = 0; e < 4; ++e) z[e] = swap16_add(u[e], u[e + 4]);
    LAS float* rec = (LAS float*)(L + L_MRG + (wi * 2 + (r >> 1)) * 528);
    *(LAS f32x4*)(rec + 4 + 8 * c + 4 * (r & 1)) = z;
    if (lane == 0 || lane == 2) *(LAS f32x2*)(L + L_MRG + (wi * 2 + (lane >> 1)) * 528) = f32x2{M, Ls};
  }
  BARRIER();
  if (wave == 1) STAMP(5);
  const unsigned tag = __builtin_amdgcn_readfirstlane(ctrl[3]);
  if (wave == 1 || wave == 2) {
    const int h = wave - 1;
    f32x2 ml[NC], ov[NC];
#pragma unroll
    for (int w8 = 0; w8 < NC; ++w8) {
      ml[w8] = *(LAS f32x2*)(L + L_MRG + (w8 * 2 + h) * 528);
      ov[w8] = *(LAS f32x2*)(L + L_MRG + (w8 * 2 + h) * 528 + 16 + lane * 8);
    }
    float MM = ml[0][0];
#pragma unroll
    for (int w8 = 1; w8 < NC; ++w8) MM = fmaxf(MM, ml[w8][0]);
    float LL = 0.f, o0 = 0.f, o1 = 0.f;
#pragma unroll
    for (int w8 = 0; w8 < NC; ++w8) {
      const float ww = __builtin_amdgcn_exp2f(ml[w8][0] - MM);
      LL = fmaf(ww, ml[w8][1], LL); o0 = fmaf(ww, ov[w8][0], o0); o1 = fmaf(ww, ov[w8][1], o1);
    }
    __builtin_amdgcn_raw_buffer_store_b128(u32x4{uf(o0), tag, uf(o1), tag}, rws, W_P + g * 16384 + (4 * h + (lane >> 4)) * 2048 + j * 256 + (lane & 15) * 16, 0, AUX_SC1);
    if (lane == 0) __builtin_amdgcn_raw_buffer_store_b128(u32x4{uf(MM), tag, uf(LL), tag}, rws, W_LSE + g * 256 + h * 128 + j * 16, 0, AUX_SC1);
  }
  if (wave == 3) {
    const int s = lane & 7, dq = lane >> 3;
    u32x4 v0, v1, vl;
#pragma nounroll
    for (int sp = 0; sp < GSPIN_CAP; ++sp) {
      v0 = __builtin_amdgcn_raw_buffer_load_b128(rws, W_P + g * 16384 + j * 2048 + s * 256 + dq * 32, 0, AUX_SC1);
      v1 = __builtin_amdgcn_raw_buffer_load_b128(rws, W_P + g * 16384 + j * 2048 + s * 256 + dq * 32 + 16, 0, AUX_SC1);
      vl = __builtin_amdgcn_raw_buffer_load_b128(rws, W_LSE + g * 256 + (j >> 2) * 128 + s * 16, 0, AUX_SC1);
      const bool ok = (v0[1] == tag) & (v0[3] == tag) & (v1[1] == tag) & (v1[3] == tag) & (vl[1] == tag) & (vl[3] == tag);
      if (__all(ok)) break;
      asm volatile("" ::: "memory");
    }
    STAMP(6);
    const float ms = fu(vl[0]);
    float MM = fmaxf(ms, dpp<XOR1>(ms)); MM = fmaxf(MM, dpp<XOR2>(MM)); MM = fmaxf(MM, dpp<XOR7>(MM));
    const float ww = __builtin_amdgcn_exp2f(ms - MM);
    float t[5] = {ww * fu(vl[2]), ww * fu(v0[0]), ww * fu(v0[2]), ww * fu(v1[0]), ww * fu(v1[2])};
#pragma unroll
    for (int e = 0; e < 5; ++e) { t[e] += dpp<XOR1>(t[e]); t[e] += dpp<XOR2>(t[e]); t[e] += dpp<XOR7>(t[e]); }
    const float inv = 1.0f / t[0];
    const unsigned b01 = pack_bf16(t[1] * inv, t[2] * inv), b23 = pack_bf16(t[3] * inv, t[4] * inv);
    if (s == 0) *(LAS f32x4*)(L + L_OBUF + dq * 16) = f32x4{bflo(b01), bfhi(b01), bflo(b23), bfhi(b23)};
  }
  BARRIER();
  {
    const f32x4 o4 = *(LAS f32x4*)(L + L_OBUF + (lane >> 3) * 16);
    float a[8];
#pragma unroll
    for (int e = 0; e < 8; ++e) a[e] = 0.f;
#pragma unroll
    for (int i = 0; i < 4; ++i)
#pragma unroll
      for (int e = 0; e < 4; ++e) { a[2 * e] = fmaf(o4[i], bflo(wo[i][e]), a[2 * e]); a[2 * e + 1] = fmaf(o4[i], bfhi(wo[i][e]), a[2 * e + 1]); }
    float u4[4], u2[2];
#pragma unroll
    for (int e = 0; e < 4; ++e) u4[e] = swap32_add(a[e], a[e + 4]);
#pragma unroll
    for (int e = 0; e < 2; ++e) u2[e] = swap16_add(u4[e], u4[e + 2]);
    const float t0 = u2[0] + dpp<XOR8>(u2[0]), t1 = u2[1] + dpp<XOR8>(u2[1]);
    const int pid = kvh * 8 + j;
    if (!(lane & 8))
      __builtin_amdgcn_raw_buffer_store_b128(u32x4{uf(t0), tag, uf(t1), tag}, rws, W_O + (br * 128 + pid) * 4608 + (32 * wave + 4 * (lane & 7) + (lane >> 4)) * 16, 0, AUX_SC1);
    if (wave == 3) STAMP(7);
    if (wave == 4 && pid < 72) {
      const int pp = lane >> 2, uq = lane & 3;
      u32x4 v[8];
#pragma nounroll
      for (int sp = 0; sp < GSPIN_CAP; ++sp) {
        bool ok = true;
#pragma unroll
        for (int k = 0; k < 8; ++k) v[k] = __builtin_amdgcn_raw_buffer_load_b128(rws, W_O + (br * 128 + 16 * k + pp) * 4608 + (pid * 4 + uq) * 16, 0, AUX_SC1);
#pragma unroll
        for (int k = 0; k < 8; ++k) ok &= (v[k][1] == tag) & (v[k][3] == tag);
        if (__all(ok)) break;
        asm volatile("" ::: "memory");
      }
      STAMP(8);
      float s0 = 0.f, s1 = 0.f;
#pragma unroll
      for (int k = 0; k < 8; ++k) { s0 += fu(v[k][0]); s1 += fu(v[k][2]); }
      s0 += dpp<ROR4>(s0); s1 += dpp<ROR4>(s1);
      s0 += dpp<XOR8>(s0); s1 += dpp<XOR8>(s1);
      s0 = swap16_add(s0, s0); s1 = swap16_add(s1, s1);
      s0 = swap32_add(s0, s0); s1 = swap32_add(s1, s1);
      if (lane < 4) ((unsigned*)p.out)[br * 288 + pid * 4 + lane] = pack_bf16(s0, s1);
      if (pid == 0 && lane == 0) __builtin_amdgcn_raw_buffer_store_b32(tag, rws, W_EPOCH + br * 256, 0, AUX_SC1);
      STAMP(9);
    }
  }
}

extern "C" void kernel_launch(void* const* d_in, const int* in_sizes, int n_in, void* d_out, int out_size, void* d_ws, size_t ws_size, hipStream_t stream) {
  Params p;
  for (int i = 0; i < 10; ++i) p.in[i] = (const unsigned short*)d_in[i];
  p.out = (unsigned short*)d_out;
  p.ws = (unsigned char*)d_ws;
  (void)hipFuncSetAttribute((const void*)attn_fused, hipFuncAttributeMaxDynamicSharedMemorySize, L_TOTAL);
  attn_fused<<<256, 576, L_TOTAL, stream>>>(p);
}
```

```cpp
#define WSOFF (((ws_size / 2) >> 12) << 12)
#include <hip/hip_runtime.h>
#include <stdint.h>

typedef unsigned u32x4 __attribute__((ext_vector_type(4)));
typedef unsigned u32x2 __attribute__((ext_vector_type(2)));
typedef float f32x4 __attribute__((ext_vector_type(4)));
typedef float f32x2 __attribute__((ext_vector_type(2)));
typedef __bf16 bf16x2 __attribute__((ext_vector_type(2)));
#define LAS __attribute__((address_space(3)))

#define TAU0 14100
#define TAU1 TAU0
#define TAU2 TAU1
#define TAU3 14300
#define ANCH 45000
#define WIN 48
#define WP 20
#define WT 24
#define MIDSLEEP 6
#define QDELAY 40
#define H1DELAY 24
#define H2DELAY 32

constexpr int DM = 576;
constexpr int RINGB = 36 * 4096;
constexpr int UQ = 9;
constexpr int UKV = 256;
constexpr int UWO = UQ + UKV;
constexpr int L_WQ = 27 * 4096, L_WO = (UKV % 36) * 4096;
static_assert(UKV % 36 + 9 <= 36, "w_o image must not wrap");
static_assert(WIN % 4 == 0 && WIN <= 60 && WIN >= 36 && WT % 4 == 0 && WT <= 36 && WP % 4 == 0, "vmcnt is 6 bits; units are 4 loads");
constexpr int L_CTRL = RINGB;
constexpr int C_LANDED = 0, C_QCNT = 1, C_QREADY = 2, C_TAG = 3, C_KCNT = 4, C_LCNT = 6, C_PROG = 8, C_RCNT = 16, C_PUB = 18;
constexpr int L_STAT = L_CTRL + 256;
constexpr int L_QBUF = L_STAT + 256;
constexpr int L_MRG = L_QBUF + 1024;
constexpr int L_QPART = L_MRG;
constexpr int L_OBUF = L_MRG + 8192;
constexpr int L_TOTAL = L_OBUF + 128;
constexpr int W_EPOCH = 0;
constexpr int W_LSE = 4096;
constexpr int W_Q = 32768;
constexpr int W_P = W_Q + 16 * 16384;
constexpr int W_O = W_P + 16 * 65536;
constexpr int W_END = W_O + 2 * 128 * 4608;
constexpr int SPIN_CAP = 1 << 19;
constexpr int GSPIN_CAP = 1 << 16;

struct Params {
  const unsigned short *ka, *va, *kb, *vb, *wqa, *wqb, *woa, *wob, *xa, *xb;
  unsigned short* out;
  unsigned char* ws;
};

__device__ __forceinline__ float dot2(unsigned k, unsigned q, float acc) {
  return __builtin_amdgcn_fdot2_f32_bf16(__builtin_bit_cast(bf16x2, k), __builtin_bit_cast(bf16x2, q), acc, false);
}
template <int CTRL> __device__ __forceinline__ float dpp(float x) {
  return __builtin_bit_cast(float, __builtin_amdgcn_mov_dpp(__builtin_bit_cast(int, x), CTRL, 0xf, 0xf, true));
}
__device__ __forceinline__ float bflo(unsigned u) { return __builtin_bit_cast(float, u << 16); }
__device__ __forceinline__ float bfhi(unsigned u) { return __builtin_bit_cast(float, u & 0xffff0000u); }
__device__ __forceinline__ unsigned pack_bf16(float lo, float hi) {
  bf16x2 v = {(__bf16)lo, (__bf16)hi};
  return __builtin_bit_cast(unsigned, v);
}
__device__ __forceinline__ float fu(unsigned u) { return __builtin_bit_cast(float, u); }
__device__ __forceinline__ unsigned uf(float f) { return __builtin_bit_cast(unsigned, f); }
constexpr int XOR1 = 0xB1, XOR2 = 0x4E, XOR7 = 0x141, XOR8 = 0x128, ROR4 = 0x124, BCAST = 0x150;
__device__ __forceinline__ float swap32_add(float a, float b) {
  auto s = __builtin_amdgcn_permlane32_swap(uf(a), uf(b), false, false);
  return fu(s[0]) + fu(s[1]);
}
__device__ __forceinline__ float swap16_add(float a, float b) {
  auto s = __builtin_amdgcn_permlane16_swap(uf(a), uf(b), false, false);
  return fu(s[0]) + fu(s[1]);
}
__device__ __forceinline__ float xrow_max(float x) {
  auto s = __builtin_amdgcn_permlane16_swap(uf(x), uf(x), false, false);
  x = fmaxf(fu(s[0]), fu(s[1]));
  auto t = __builtin_amdgcn_permlane32_swap(uf(x), uf(x), false, false);
  return fmaxf(fu(t[0]), fu(t[1]));
}
#define BARRIER() asm volatile("s_waitcnt lgkmcnt(0)\n\ts_barrier" ::: "memory")
#define MEMBAR() asm volatile("" ::: "memory")
#define LGKM0() asm volatile("s_waitcnt lgkmcnt(0)" ::: "memory")
constexpr int SC1 = 16;
constexpr unsigned TAGX = 0xA5C3965Au;
__device__ __forceinline__ u32x4 pair(float a, float b, unsigned tag) { return u32x4{uf(a), tag, uf(b), tag ^ TAGX}; }

__device__ __forceinline__ void wait_gt(volatile LAS unsigned* w, unsigned x) {
#pragma nounroll
  for (int sp = 0; sp < SPIN_CAP; ++sp) {
    if (__builtin_amdgcn_readfirstlane(*w) > x) break;
    __builtin_amdgcn_s_sleep(1);
  }
  MEMBAR();
}
template <int N, int PAUSE>
__device__ __forceinline__ void gather(const __amdgpu_buffer_rsrc_t rws, int off, int stride, unsigned tag, u32x4 (&v)[N]) {
  bool ok = false;
#pragma nounroll
  for (int sp = 0; sp < GSPIN_CAP; ++sp) {
    if (!ok) {
#pragma unroll
      for (int k = 0; k < N; ++k) v[k] = __builtin_amdgcn_raw_buffer_load_b128(rws, off + k * stride, 0, SC1);
      ok = true;
#pragma unroll
      for (int k = 0; k < N; ++k) ok &= (v[k][1] == tag) & (v[k][3] == (tag ^ TAGX));
    }
    if (__all(ok)) break;
    if (PAUSE) __builtin_amdgcn_s_sleep(PAUSE);
    MEMBAR();
  }
}

#define UNITW(w, va, vb, vc, vd) "s_waitcnt vmcnt(%[" w "])\n\tds_max_i32 %[vpa], %[vpub]\n\t" \
      "buffer_load_dwordx4 %[" va "], s[40:43], %[sso] offen nt lds\n\t" \
      "buffer_load_dwordx4 %[" vb "], s[40:43], %[sso] offen offset:1024 nt lds\n\t" \
      "buffer_load_dwordx4 %[" vc "], s[40:43], %[sso] offen offset:2048 nt lds\n\t" \
      "buffer_load_dwordx4 %[" vd "], s[40:43], %[sso] offen offset:3072 nt lds\n\t" \
      "s_add_u32 m0, m0, 4096\n\tv_add_u32 %[vpub], 1, %[vpub]\n\t"
#define UNIT(v) UNITW("WM4", v, v, v, v)
#define ROT(v) "v_add_u32 v60, 0x4000, %[" v "]\n\tv_bfi_b32 %[" v "], s23, v60, %[" v "]\n\t"

extern "C" __global__ void __launch_bounds__(640, 3) attn_fused(Params p) {
  extern __shared__ __attribute__((aligned(16))) unsigned char smem[];
  LAS unsigned char* L = (LAS unsigned char*)smem;
  const int tid = threadIdx.x, wave = __builtin_amdgcn_readfirstlane(tid >> 6);
  const int bid = blockIdx.x;
  const int wi16 = bid >> 4, kvh = bid & 15;
  const int br = wi16 >> 3, pid = kvh * 8 + (wi16 & 7);
  const int qb = (bid >> 2) & 1, rq = bid & 3, qh = bid >> 3;
  {
    const unsigned ldsbase = (unsigned)(uintptr_t)L;
    unsigned v0, v1, v2, v3, vctrl, vprog, vpub, vpa;
    unsigned sn, sslot, sso, sdue, st1, sspin;
    asm volatile(
      "s_load_dwordx16 s[24:39], %[karg], 0x0\n\t"
      "s_memrealtime s[20:21]\n\t"
      "v_mov_b32 v62, 0\n\t"
      "v_mov_b32 %[vctrl], %[ctrl]\n\t"
      "v_lshl_add_u32 v59, %[tid], 2, %[vctrl]\n\t"
      "s_cmp_eq_u32 %[wave], 0\n\t"
      "s_cbranch_scc1 .Lload%=\n\t"
      "s_barrier\n\t"
      "s_waitcnt lgkmcnt(0)\n\t"
      "s_branch .Lend%=\n\t"
      ".Lload%=:\n\t"
      "ds_write_b32 v59, v62\n\t"
      "s_setprio 3\n\t"
      "v_lshrrev_b32 v60, 4, %[tid]\n\t"
      "v_and_b32 v61, 15, %[tid]\n\t"
      "v_lshlrev_b32 v60, 13, v60\n\t"
      "v_lshl_or_b32 %[v0], v61, 4, v60\n\t"
      "v_add_u32 %[v1], 0x7c00, %[v0]\n\t"
      "v_add_u32 %[v2], 0xf800, %[v0]\n\t"
      "v_add_u32 %[v3], 0x17400, %[v0]\n\t"
      "s_mov_b32 %[sso], %[qoff]\n\t"
      "v_and_b32 v61, 3, %[tid]\n\t"
      "s_mov_b32 s42, 0x480000\n\t"
      "s_mov_b32 s43, 0x20000\n\t"
      "s_add_u32 m0, %[ldsbase], %[WQo]\n\t"
      "s_mov_b32 %[sn], 0\n\t"
      "v_mov_b32 %[vpub], %[PUB0]\n\t"
      "v_lshl_add_u32 %[vpa], %[tid], 20, %[vctrl]\n\t"
      "v_lshl_add_u32 %[vprog], v61, 2, %[vctrl]\n\t"
      "s_cmp_eq_u32 %[qb], 0\n\t"
      "s_waitcnt lgkmcnt(0)\n\t"
      "s_cselect_b64 s[40:41], s[32:33], s[34:35]\n\t"
      ".Lwq%=:\n\t"
      UNITW("WM4", "v0", "v1", "v2", "v3")
      "s_add_u32 %[sso], %[sso], 0x20000\n\t"
      "s_add_u32 %[sn], %[sn], 1\n\t"
      "s_cmp_lt_u32 %[sn], 9\n\t"
      "s_cbranch_scc1 .Lwq%=\n\t"
      "s_barrier\n\t"
      "s_lshl_b32 %[sdue], s20, 8\n\t"
      "s_add_u32 %[sdue], %[sdue], %[ANCc]\n\t"
      "s_lshl_b32 %[st1], %[wi16], 12\n\t"
      "v_lshl_add_u32 %[v0], %[tid], 4, %[st1]\n\t"
      "s_mov_b32 s23, 0xffff\n\t"
      "v_add_u32 v60, 0x5000, %[v0]\n\t"
      "v_add_u32 v61, 0xa000, %[v0]\n\t"
      "v_add_u32 v62, 0xf000, %[v0]\n\t"
      "v_and_b32 v60, s23, v60\n\t"
      "v_and_b32 v61, s23, v61\n\t"
      "v_and_b32 v62, s23, v62\n\t"
      "v_or_b32 %[v1], 0x10000, v60\n\t"
      "v_or_b32 %[v2], 0x20000, v61\n\t"
      "v_or_b32 %[v3], 0x30000, v62\n\t"
      "s_cmp_lg_u32 %[br], 0\n\t"
      "s_cselect_b64 s[36:37], s[38:39], s[36:37]\n\t"
      "s_mov_b32 s42, 0x4000000\n\t"
      "s_mov_b32 %[sn], 0\n\t"
      "s_mov_b32 %[sslot], %[ldsbase]\n\t"
      "s_mov_b32 s19, 0\n\t"
      "s_mov_b32 s16, %[T01]\n\t"
      "s_mov_b32 s17, %[T23]\n\t"
      "s_waitcnt vmcnt(20)\n\t"
      "s_movk_i32 %[sspin], 100\n\t"
      ".Lp0%=:\n\t"
      "s_memrealtime s[20:21]\n\t"
      "s_sub_u32 %[sspin], %[sspin], 1\n\t"
      "s_waitcnt lgkmcnt(0)\n\t"
      "s_cbranch_scc1 .Ltensor%=\n\t"
      "s_lshl_b32 s22, s20, 8\n\t"
      "s_sub_u32 s22, s22, %[sdue]\n\t"
      "s_cmp_lt_i32 s22, 0\n\t"
      "s_cbranch_scc1 .Lp0%=\n\t"
      ".Ltensor%=:\n\t"
      "s_mov_b64 s[40:41], s[24:25]\n\t"
      "s_mov_b64 s[24:25], s[26:27]\n\t"
      "s_mov_b64 s[26:27], s[28:29]\n\t"
      "s_mov_b64 s[28:29], s[30:31]\n\t"
      "s_lshl_b32 %[sso], %[kvh], 22\n\t"
      "s_and_b32 s18, s16, 0xffff\n\t"
      "s_lshr_b64 s[16:17], s[16:17], 16\n\t"
      ".Lmain%=:\n\t"
      "s_mov_b32 m0, %[sslot]\n\t"
      "ds_read_b32 v61, %[vprog] offset:32\n\t"
      UNIT("v0") UNIT("v1")
      "s_cmp_eq_u32 s19, 0\n\t"
      "s_cbranch_scc1 .Lnomid%=\n\t"
      "s_sleep %c[MS]\n\t"
      "s_mov_b32 s19, 0\n\t"
      ".Lnomid%=:\n\t"
      UNIT("v2") UNIT("v3")
      "s_memrealtime s[20:21]\n\t"
      ROT("v0") ROT("v1") ROT("v2") ROT("v3")
      "s_add_u32 %[sso], %[sso], 0x40000\n\t"
      "s_add_u32 %[sslot], %[sslot], 16384\n\t"
      "s_cmp_lt_u32 %[sslot], %[ctrl]\n\t"
      "s_cselect_b32 %[sslot], %[sslot], %[ldsbase]\n\t"
      "s_sub_u32 %[st1], %[sn], 8\n\t"
      "s_ashr_i32 %[st1], %[st1], 1\n\t"
      "s_add_u32 %[sn], %[sn], 1\n\t"
      "s_add_u32 %[sdue], %[sdue], s18\n\t"
      "s_cmp_eq_u32 %[sn], 1\n\t"
      "s_cbranch_scc1 .Lwqpub%=\n\t"
      ".Lsched%=:\n\t"
      "s_waitcnt lgkmcnt(0)\n\t"
      "v_cmp_ge_i32 vcc, %[st1], v61\n\t"
      "s_lshl_b32 s22, s20, 8\n\t"
      "s_sub_u32 s22, s22, %[sdue]\n\t"
      "s_cmp_lt_i32 s22, 0\n\t"
      "s_cbranch_scc1 .Lpace%=\n\t"
      ".Lpaced%=:\n\t"
      "s_cbranch_vccnz .Lfull%=\n\t"
      ".Lfree%=:\n\t"
      "v_xor_b32 %[vprog], 16, %[vprog]\n\t"
      "s_cmp_eq_u32 %[sn], 6\n\t"
      "s_cbranch_scc1 .Lqchk%=\n\t"
      "s_and_b32 %[st1], %[sn], 15\n\t"
      "s_cbranch_scc1 .Lmain%=\n\t"
      "s_cmp_lt_u32 %[sn], 64\n\t"
      "s_cbranch_scc1 .Ltensor%=\n\t"
      "v_and_b32 v60, 7, %[tid]\n\t"
      "v_lshl_add_u32 %[vprog], v60, 2, %[vctrl]\n\t"
      "s_mov_b32 %[sspin], %[CAP]\n\t"
      ".Lwochk%=:\n\t"
      "ds_read_b32 v61, %[vprog] offset:32\n\t"
      "s_sub_u32 %[sspin], %[sspin], 1\n\t"
      "s_waitcnt lgkmcnt(0)\n\t"
      "s_cbranch_scc1 .Ldrain%=\n\t"
      "v_cmp_ge_i32 vcc, 28, v61\n\t"
      "s_cbranch_vccz .Lwogo%=\n\t"
      "v_mov_b32 v58, %[UWOc]\n\t"
      "s_waitcnt vmcnt(0)\n\t"
      "ds_max_i32 %[vpa], v58\n\t"
      "s_branch .Lwochk%=\n\t"
      ".Lwogo%=:\n\t"
      "s_mul_i32 %[sso], %[pid], 0x9000\n\t"
      "s_mov_b32 s42, 0x480000\n\t"
      "s_mov_b64 s[40:41], s[36:37]\n\t"
      "v_lshlrev_b32 %[v0], 4, %[tid]\n\t"
      "s_mov_b32 m0, %[sslot]\n\t"
      "s_mov_b32 %[sn], 0\n\t"
      "v_add_u32 %[vpub], %[DWT], %[vpub]\n\t"
      ".Lwo%=:\n\t"
      UNITW("WT4", "v0", "v0", "v0", "v0")
      "s_add_u32 %[sso], %[sso], 0x1000\n\t"
      "s_add_u32 %[sn], %[sn], 1\n\t"
      "s_cmp_lt_u32 %[sn], 9\n\t"
      "s_cbranch_scc1 .Lwo%=\n\t"
      "s_branch .Ldrain%=\n\t"
      ".Lwqpub%=:\n\t"
      "v_mov_b32 v58, 9\n\t"
      "s_waitcnt vmcnt(16) lgkmcnt(0)\n\t"
      "ds_max_i32 %[vpa], v58\n\t"
      "s_memrealtime s[20:21]\n\t"
      "s_waitcnt lgkmcnt(0)\n\t"
      "s_lshl_b32 s22, s20, 8\n\t"
      "s_sub_u32 %[sspin], s22, %[sdue]\n\t"
      "s_cmp_gt_i32 %[sspin], 0\n\t"
      "s_cselect_b32 %[sdue], s22, %[sdue]\n\t"
      "s_branch .Lsched%=\n\t"
      ".Lpace%=:\n\t"
      "s_cmp_ge_u32 %[sn], 64\n\t"
      "s_cbranch_scc1 .Lpaced%=\n\t"
      "s_movk_i32 %[sspin], 100\n\t"
      ".Lpace1%=:\n\t"
      "s_memrealtime s[20:21]\n\t"
      "s_sub_u32 %[sspin], %[sspin], 1\n\t"
      "s_waitcnt lgkmcnt(0)\n\t"
      "s_cbranch_scc1 .Lpaced%=\n\t"
      "s_lshl_b32 s22, s20, 8\n\t"
      "s_sub_u32 s22, s22, %[sdue]\n\t"
      "s_cmp_lt_i32 s22, 0\n\t"
      "s_cbranch_scc1 .Lpace1%=\n\t"
      "s_mov_b32 s19, 1\n\t"
      "v_add_u32 v58, %[LAGP], %[vpub]\n\t"
      "s_waitcnt vmcnt(%[WPc])\n\t"
      "ds_max_i32 %[vpa], v58\n\t"
      "s_branch .Lpaced%=\n\t"
      ".Lfull%=:\n\t"
      "ds_read_b32 v61, %[vprog] offset:32\n\t"
      "s_waitcnt lgkmcnt(0)\n\t"
      "v_cmp_ge_i32 vcc, %[st1], v61\n\t"
      "s_cbranch_vccz .Lfree%=\n\t"
      "v_add_u32 v59, %[LAGc], %[vpub]\n\t"
      "s_waitcnt vmcnt(0)\n\t"
      "ds_max_i32 %[vpa], v59\n\t"
      "s_mov_b32 %[sspin], %[CAP]\n\t"
      ".Lspin%=:\n\t"
      "ds_read_b32 v61, %[vprog] offset:32\n\t"
      "s_sub_u32 %[sspin], %[sspin], 1\n\t"
      "s_waitcnt lgkmcnt(0)\n\t"
      "s_cbranch_scc1 .Ldrain%=\n\t"
      "v_cmp_ge_i32 vcc, %[st1], v61\n\t"
      "s_cbranch_vccnz .Lspin%=\n\t"
      "s_branch .Lfree%=\n\t"
      ".Lqchk%=:\n\t"
      "s_mov_b32 %[sspin], %[CAP]\n\t"
      ".Lqchk1%=:\n\t"
      "ds_read_b32 v61, %[vctrl] offset:4\n\t"
      "s_sub_u32 %[sspin], %[sspin], 1\n\t"
      "s_waitcnt lgkmcnt(0)\n\t"
      "s_cbranch_scc1 .Ldrain%=\n\t"
      "v_cmp_ge_i32 vcc, 7, v61\n\t"
      "s_cbranch_vccnz .Lqchk1%=\n\t"
      "s_branch .Lmain%=\n\t"
      ".Ldrain%=:\n\t"
      "v_mov_b32 v62, 0x40000000\n\t"
      "s_waitcnt vmcnt(0)\n\t"
      "ds_write_b32 %[vpa], v62\n\t"
      "s_setprio 0\n\t"
      "s_waitcnt lgkmcnt(0)\n\t"
      ".Lend%=:\n\t"
      : [v0] "=&v"(v0), [v1] "=&v"(v1), [v2] "=&v"(v2), [v3] "=&v"(v3), [vctrl] "=&v"(vctrl), [vprog] "=&v"(vprog), [vpub] "=&v"(vpub), [vpa] "=&v"(vpa),
        [sn] "=&s"(sn), [sslot] "=&s"(sslot), [sso] "=&s"(sso), [sdue] "=&s"(sdue), [st1] "=&s"(st1), [sspin] "=&s"(sspin)
      : [tid] "v"(tid), [wave] "s"(wave), [karg] "s"(__builtin_amdgcn_kernarg_segment_ptr()), [kvh] "s"(kvh), [pid] "s"(pid), [br] "s"(br), [wi16] "s"(wi16),
        [qb] "s"(qb), [qoff] "s"(rq * (144 * 8192) + qh * 256), [ldsbase] "s"(ldsbase), [ctrl] "s"(ldsbase + L_CTRL),
        [WM4] "n"(WIN - 4), [WT4] "n"(WT - 4), [DWT] "n"((WIN - WT) / 4), [PUB0] "n"(1 - WIN / 4), [LAGc] "n"(WIN / 4 - 1), [LAGP] "n"(WIN / 4 - 1 - WP / 4),
        [WPc] "n"(WP), [UWOc] "n"(UWO), [WQo] "n"(L_WQ), [MS] "n"(MIDSLEEP), [CAP] "n"(SPIN_CAP), [T01] "n"(TAU0 | (TAU1 << 16)), [T23] "n"(TAU2 | (TAU3 << 16)), [ANCc] "n"(ANCH)
      : "memory", "scc", "vcc", "m0", "s16", "s17", "s18", "s19", "s20", "s21", "s22", "s23", "s24", "s25", "s26", "s27", "s28", "s29", "s30", "s31", "s32", "s33", "s34", "s35",
        "s36", "s37", "s38", "s39", "s40", "s41", "s42", "s43", "v58", "v59", "v60", "v61", "v62");
  }
  const int lane = tid & 63;
  volatile LAS unsigned* ctrl = (volatile LAS unsigned*)(L + L_CTRL);
  LAS unsigned* actrl = (LAS unsigned*)(L + L_CTRL);
  const __amdgpu_buffer_rsrc_t rws = __builtin_amdgcn_make_buffer_rsrc((void*)p.ws, 0, W_END, 0x00020000);

  if (wave == 9) {
    unsigned tag;
    asm volatile("s_load_dword %0, %1, 0x0\n\ts_waitcnt lgkmcnt(0)" : "=&s"(tag) : "s"(p.ws + W_EPOCH) : "memory");
    tag += 1u;
    if (lane == 0) ctrl[C_TAG] = tag;
    {
      wait_gt(ctrl + C_QCNT, 7);
      f32x2 s2 = {0.f, 0.f};
#pragma unroll
      for (int w8 = 0; w8 < 8; ++w8) s2 += *(LAS f32x2*)(L + L_QPART + w8 * 512 + lane * 8);
      __builtin_amdgcn_raw_buffer_store_b128(pair(s2[0], s2[1], tag), rws, W_Q + (qh >> 1) * 16384 + qb * 8192 + rq * 2048 + (qh & 1) * 1024 + lane * 16, 0, SC1);
      __builtin_amdgcn_s_sleep(QDELAY);
#pragma nounroll
      for (int b = 0; b < 2; ++b) {
        u32x4 v[8];
        gather<8, 10>(rws, W_Q + kvh * 16384 + b * 8192 + lane * 16, 1024, tag, v);
#pragma unroll
        for (int h = 0; h < 2; ++h)
          ((LAS unsigned*)(L + L_QBUF))[b * 128 + h * 64 + lane] = pack_bf16((fu(v[h][0]) + fu(v[h + 2][0])) + (fu(v[h + 4][0]) + fu(v[h + 6][0])),
                                                                             (fu(v[h][2]) + fu(v[h + 2][2])) + (fu(v[h + 4][2]) + fu(v[h + 6][2])));
        LGKM0();
        if (lane == 0) ctrl[C_QREADY] = b + 1;
      }
    }
    {
      const int s = lane & 15, dq = lane >> 4;
      wait_gt(ctrl + C_LANDED, UWO - 24);
      u32x4 vl[1];
      gather<1, 0>(rws, W_LSE + kvh * 1024 + (wi16 >> 2) * 256 + s * 16, 0, tag, vl);
      const float ms = fu(vl[0][0]);
      float MM = fmaxf(ms, dpp<XOR1>(ms)); MM = fmaxf(MM, dpp<XOR2>(MM)); MM = fmaxf(MM, dpp<XOR7>(MM)); MM = fmaxf(MM, dpp<XOR8>(MM));
      float ww = __builtin_amdgcn_exp2f(ms - MM);
      float tl = ww * fu(vl[0][2]);
      tl += dpp<XOR1>(tl); tl += dpp<XOR2>(tl); tl += dpp<XOR7>(tl); tl += dpp<XOR8>(tl);
      ww = ww / tl;
      asm volatile("" : "+v"(ww));
#pragma nounroll
      for (int sp = 0; ctrl[C_PUB + br] == 0u && sp < SPIN_CAP; ++sp) {}
      MEMBAR();
      __builtin_amdgcn_s_sleep(H1DELAY);
      u32x4 v[4];
      gather<4, 0>(rws, W_P + kvh * 65536 + wi16 * 4096 + s * 256 + dq * 16, 64, tag, v);
      float t[8];
#pragma unroll
      for (int k = 0; k < 4; ++k) { t[2 * k] = ww * fu(v[k][0]); t[2 * k + 1] = ww * fu(v[k][2]); }
#define TR_STEP(HALF, CTRL, BIT) _Pragma("unroll") for (int h = 0; h < (HALF); ++h) { \
        const float lo_ = t[h], hi_ = t[h + (HALF)]; const float t1_ = lo_ + dpp<CTRL>(lo_), t2_ = hi_ + dpp<CTRL>(hi_); \
        t[h] = (s & (BIT)) ? t2_ : t1_; }
      TR_STEP(4, XOR8, 8) TR_STEP(2, XOR7, 4) TR_STEP(1, XOR2, 2)
#undef TR_STEP
      const float ov = t[0] + dpp<XOR1>(t[0]);
      if (!(s & 1)) ((LAS float*)(L + L_OBUF))[(s >> 1) * 4 + dq] = bflo(pack_bf16(ov, ov));
    }
    BARRIER();
    if (pid < 72) {
      __builtin_amdgcn_s_sleep(H2DELAY);
      u32x4 v[8];
      gather<8, 0>(rws, W_O + (br * 128 + 8 * (lane >> 2)) * 4608 + (pid * 4 + (lane & 3)) * 16, 4608, tag, v);
      asm volatile(".Lattn_tail:");
      float s0 = 0.f, s1 = 0.f;
#pragma unroll
      for (int k = 0; k < 8; ++k) { s0 += fu(v[k][0]); s1 += fu(v[k][2]); }
      s0 += dpp<ROR4>(s0); s1 += dpp<ROR4>(s1);
      s0 += dpp<XOR8>(s0); s1 += dpp<XOR8>(s1);
      s0 = swap16_add(s0, s0); s1 = swap16_add(s1, s1);
      s0 = swap32_add(s0, s0); s1 = swap32_add(s1, s1);
      if (lane < 4) __builtin_nontemporal_store(pack_bf16(s0, s1), (unsigned*)p.out + br * 288 + (pid >> 3) * 32 + ((pid & 1) * 4 + lane) * 4 + ((pid >> 1) & 3));
      if (bid == 0 && lane == 0) __builtin_amdgcn_raw_buffer_store_b32(tag, rws, W_EPOCH, 0, SC1);
    }
    return;
  }

  if (wave != 0) {
    const int wi = wave - 1;
    u32x4 pf, pf2;
    {
      unsigned long long pc;
      unsigned so, sz;
      asm volatile("s_getpc_b64 %0\n\t.Lpc%=:\n\ts_mov_b32 %1, .Lpc%=-attn_fused\n\ts_mov_b32 %2, .Lattn_tail-attn_fused" : "=s"(pc), "=s"(so), "=s"(sz));
      const __amdgpu_buffer_rsrc_t rc = __builtin_amdgcn_make_buffer_rsrc((void*)(pc - so), 0, sz, 0x00020000);
      pf = __builtin_amdgcn_raw_buffer_load_b128(rc, wave * 1024 + lane * 16, 0, 0);
      pf2 = __builtin_amdgcn_raw_buffer_load_b128(rc, 8192 + wave * 1024 + lane * 16, 0, 0);
    }
    {
      const __amdgpu_buffer_rsrc_t rx = __builtin_amdgcn_make_buffer_rsrc((void*)((qb ? p.xb : p.xa) + rq * 144), 0, 144 * 2, 0x00020000);
      unsigned short xv[5];
#pragma unroll
      for (int k = 0; k < 5; ++k) xv[k] = __builtin_amdgcn_raw_buffer_load_b16(rx, (4 * wi + (lane >> 4) + 32 * k) * 2, 0, 0);
      __builtin_amdgcn_sched_barrier(0);
      wait_gt(ctrl + C_LANDED, UQ - 1);
      float a[8];
#pragma unroll
      for (int e = 0; e < 8; ++e) a[e] = 0.f;
#pragma unroll
      for (int k = 0; k < 5; ++k) {
        if (k == 4 && wi >= 4) break;
        const u32x4 w = *(LAS u32x4*)(L + L_WQ + k * 8192 + wi * 1024 + lane * 16);
        const float xf = bflo(xv[k]);
#pragma unroll
        for (int e = 0; e < 4; ++e) { a[2 * e] = fmaf(xf, bflo(w[e]), a[2 * e]); a[2 * e + 1] = fmaf(xf, bfhi(w[e]), a[2 * e + 1]); }
      }
      asm volatile("" :: "v"(pf), "v"(pf2));
      float u4[4], u2[2];
#pragma unroll
      for (int e = 0; e < 4; ++e) u4[e] = swap32_add(a[e], a[e + 4]);
#pragma unroll
      for (int e = 0; e < 2; ++e) u2[e] = swap16_add(u4[e], u4[e + 2]);
      *(LAS f32x2*)(L + L_QPART + wi * 512 + (lane & 15) * 32 + (lane >> 4) * 8) = f32x2{u2[0], u2[1]};
      LGKM0();
      if (lane == 0) __hip_atomic_fetch_add(actrl + C_QCNT, 1u, __ATOMIC_RELAXED, __HIP_MEMORY_SCOPE_WORKGROUP);
    }
    const int r = lane >> 4, c = lane & 15;
    constexpr float C2 = 0.088388347648318440f * 1.4426950408889634f;
    unsigned u = UQ + wi;
    int uoff = wi * 4096;
    unsigned done = 0;
#define NEXT_UNIT(R) wait_gt(ctrl + C_LANDED, u); \
      { const LAS u32x4* sp4 = (const LAS u32x4*)(L + uoff) + lane; \
        _Pragma("unroll") for (int t = 0; t < 4; ++t) R[t] = sp4[t * 64]; } \
      asm volatile("s_waitcnt lgkmcnt(0)" : "+v"(R[0]), "+v"(R[1]), "+v"(R[2]), "+v"(R[3])::"memory"); \
      ++done; if (lane == 0) ctrl[C_PROG + wi] = done; \
      __builtin_amdgcn_sched_barrier(0); \
      u += 8; uoff += 8 * 4096; if (uoff >= RINGB) uoff -= RINGB;
#pragma nounroll
    for (int b = 0; b < 2; ++b) {
      u32x4 q0, q1;
      float s0 = 0.f, s1 = 0.f, s2 = 0.f, s3 = 0.f;
#pragma nounroll
      for (int it = 0; it < 4; ++it) {
        u32x4 Ka[4], Kb[4];
        NEXT_UNIT(Ka)
        NEXT_UNIT(Kb)
        if (it == 0) {
          wait_gt(ctrl + C_QREADY, b);
          q0 = *(LAS u32x4*)(L + L_QBUF + b * 512 + c * 16); q1 = *(LAS u32x4*)(L + L_QBUF + b * 512 + 256 + c * 16);
        }
        float x[16];
#pragma unroll
        for (int t = 0; t < 8; ++t) {
          float d0 = 0.f, d1 = 0.f;
#pragma unroll
          for (int k = 0; k < 4; ++k) { const unsigned kk = t < 4 ? Ka[t & 3][k] : Kb[t & 3][k]; d0 = dot2(kk, q0[k], d0); d1 = dot2(kk, q1[k], d1); }
          x[2 * t] = d0; x[2 * t + 1] = d1;
        }
#define TR_STEP(HALF, CTRL, BIT) _Pragma("unroll") for (int h = 0; h < (HALF); ++h) { \
          const float lo_ = x[h], hi_ = x[h + (HALF)]; const float t1_ = lo_ + dpp<CTRL>(lo_), t2_ = hi_ + dpp<CTRL>(hi_); \
          x[h] = (c & (BIT)) ? t2_ : t1_; }
        TR_STEP(8, XOR8, 8) TR_STEP(4, XOR7, 4) TR_STEP(2, XOR2, 2) TR_STEP(1, XOR1, 1)
#undef TR_STEP
        s0 = s1; s1 = s2; s2 = s3; s3 = x[0] * C2;
      }
      const unsigned tag = __builtin_amdgcn_readfirstlane(ctrl[C_TAG]);
      LAS float* st = (LAS float*)(L + L_STAT + b * 128);
      float mx = fmaxf(fmaxf(s0, s1), fmaxf(s2, s3));
      mx = fmaxf(mx, dpp<XOR2>(mx)); mx = fmaxf(mx, dpp<ROR4>(mx)); mx = fmaxf(mx, dpp<XOR8>(mx)); mx = xrow_max(mx);
      if (lane < 2) st[wi * 2 + lane] = mx;
      LGKM0();
      if (lane == 0) __hip_atomic_fetch_add(actrl + C_KCNT + b, 1u, __ATOMIC_RELAXED, __HIP_MEMORY_SCOPE_WORKGROUP);
      wait_gt(ctrl + C_KCNT + b, 7);
      mx = st[c];
      mx = fmaxf(mx, dpp<XOR2>(mx)); mx = fmaxf(mx, dpp<ROR4>(mx)); mx = fmaxf(mx, dpp<XOR8>(mx));
      s0 = __builtin_amdgcn_exp2f(s0 - mx); s1 = __builtin_amdgcn_exp2f(s1 - mx); s2 = __builtin_amdgcn_exp2f(s2 - mx); s3 = __builtin_amdgcn_exp2f(s3 - mx);
      float ls = (s0 + s1) + (s2 + s3);
      ls += dpp<XOR2>(ls); ls += dpp<ROR4>(ls); ls += dpp<XOR8>(ls); ls = swap16_add(ls, ls); ls = swap32_add(ls, ls);
      if (lane < 2) st[16 + wi * 2 + lane] = ls;
      LGKM0();
      unsigned arr = 0;
      if (lane == 0) arr = __hip_atomic_fetch_add(actrl + C_LCNT + b, 1u, __ATOMIC_RELAXED, __HIP_MEMORY_SCOPE_WORKGROUP);
      if (__builtin_amdgcn_readfirstlane(arr) == 7) {
        MEMBAR();
        float lt = st[16 + c];
        lt += dpp<XOR2>(lt); lt += dpp<ROR4>(lt); lt += dpp<XOR8>(lt);
        if (lane < 2) __builtin_amdgcn_raw_buffer_store_b128(pair(mx, lt, tag), rws, W_LSE + kvh * 1024 + (2 * b + lane) * 256 + wi16 * 16, 0, SC1);
      }
      float acc[2][8];
#pragma unroll
      for (int e = 0; e < 8; ++e) { acc[0][e] = 0.f; acc[1][e] = 0.f; }
#pragma nounroll
      for (int it = 0; it < 4; ++it) {
        const float pe = s0; s0 = s1; s1 = s2; s2 = s3;
#define PV(hf, t) { const float p0 = dpp<BCAST + 8 * hf + 2 * t>(pe), p1 = dpp<BCAST + 8 * hf + 2 * t + 1>(pe); \
          _Pragma("unroll") for (int k = 0; k < 4; ++k) { const float lo = bflo(V[t][k]), hi = bfhi(V[t][k]); \
            acc[0][2 * k] = fmaf(p0, lo, acc[0][2 * k]); acc[0][2 * k + 1] = fmaf(p0, hi, acc[0][2 * k + 1]); \
            acc[1][2 * k] = fmaf(p1, lo, acc[1][2 * k]); acc[1][2 * k + 1] = fmaf(p1, hi, acc[1][2 * k + 1]); } }
        { u32x4 V[4]; NEXT_UNIT(V) PV(0, 0) PV(0, 1) PV(0, 2) PV(0, 3) }
        asm volatile("" : "+v"(acc[0][0]), "+v"(acc[0][1]), "+v"(acc[0][2]), "+v"(acc[0][3]), "+v"(acc[0][4]), "+v"(acc[0][5]), "+v"(acc[0][6]), "+v"(acc[0][7]),
                          "+v"(acc[1][0]), "+v"(acc[1][1]), "+v"(acc[1][2]), "+v"(acc[1][3]), "+v"(acc[1][4]), "+v"(acc[1][5]), "+v"(acc[1][6]), "+v"(acc[1][7]));
        { u32x4 V[4];
          NEXT_UNIT(V) PV(1, 0) PV(1, 1) PV(1, 2) PV(1, 3) }
#undef PV
      }
      f32x4 o[7];
      unsigned rc = 0;
      if (wi == 7) {
        rc = ctrl[C_RCNT + b];
#pragma unroll
        for (int w8 = 0; w8 < 7; ++w8) o[w8] = *(volatile LAS f32x4*)(L + L_MRG + w8 * 1024 + lane * 16);
      }
      float y[8];
#pragma unroll
      for (int e = 0; e < 8; ++e) y[e] = swap32_add(acc[0][e], acc[1][e]);
      f32x4 z;
#pragma unroll
      for (int e = 0; e < 4; ++e) z[e] = swap16_add(y[e], y[e + 4]);
      if (wi != 7) {
        *(LAS f32x4*)(L + L_MRG + wi * 1024 + lane * 16) = z;
        LGKM0();
        if (lane == 0) __hip_atomic_fetch_add(actrl + C_RCNT + b, 1u, __ATOMIC_RELAXED, __HIP_MEMORY_SCOPE_WORKGROUP);
      } else {
        if (__builtin_amdgcn_readfirstlane(rc) < 7u) {
#pragma nounroll
          for (int sp = 0; ctrl[C_RCNT + b] < 7u && sp < SPIN_CAP; ++sp) {}
          MEMBAR();
#pragma unroll
          for (int w8 = 0; w8 < 7; ++w8) o[w8] = *(volatile LAS f32x4*)(L + L_MRG + w8 * 1024 + lane * 16);
        }
        z += ((o[0] + o[1]) + (o[2] + o[3])) + ((o[4] + o[5]) + o[6]);
        const int off = W_P + kvh * 65536 + (8 * b + 4 * (r >> 1) + (c >> 2)) * 4096 + wi16 * 256 + (r & 1) * 128 + (c & 3) * 16;
        __builtin_amdgcn_raw_buffer_store_b128(pair(z[0], z[1], tag), rws, off, 0, SC1);
        __builtin_amdgcn_raw_buffer_store_b128(pair(z[2], z[3], tag), rws, off + 64, 0, SC1);
        if (lane == 0) ctrl[C_PUB + b] = 1u;
      }
    }
  }
  {
    const unsigned tag = __builtin_amdgcn_readfirstlane(ctrl[C_TAG]);
    wait_gt(ctrl + C_LANDED, UWO + 9);
    float wf[4][8];
#pragma unroll
    for (int i = 0; i < 4; ++i) {
      const u32x4 wo = *(LAS u32x4*)(L + L_WO + ((lane >> 3) + 8 * i) * (DM * 2) + (8 * wave + (lane & 7)) * 16);
#pragma unroll
      for (int e = 0; e < 4; ++e) { wf[i][2 * e] = bflo(wo[e]); wf[i][2 * e + 1] = bfhi(wo[e]); }
    }
#pragma unroll
    for (int i = 0; i < 4; ++i)
      asm volatile("" : "+v"(wf[i][0]), "+v"(wf[i][1]), "+v"(wf[i][2]), "+v"(wf[i][3]), "+v"(wf[i][4]), "+v"(wf[i][5]), "+v"(wf[i][6]), "+v"(wf[i][7]));
    BARRIER();
    const f32x4 o4 = *(LAS f32x4*)(L + L_OBUF + (lane >> 3) * 16);
    float a[8];
#pragma unroll
    for (int e = 0; e < 8; ++e) a[e] = o4[0] * wf[0][e];
#pragma unroll
    for (int i = 1; i < 4; ++i)
#pragma unroll
      for (int e = 0; e < 8; ++e) a[e] = fmaf(o4[i], wf[i][e], a[e]);
    float u4[4], u2[2];
#pragma unroll
    for (int e = 0; e < 4; ++e) u4[e] = swap32_add(a[e], a[e + 4]);
#pragma unroll
    for (int e = 0; e < 2; ++e) u2[e] = swap16_add(u4[e], u4[e + 2]);
    const float t0 = u2[0] + dpp<XOR8>(u2[0]), t1 = u2[1] + dpp<XOR8>(u2[1]);
    if (!(lane & 8))
      __builtin_amdgcn_raw_buffer_store_b128(pair(t0, t1, tag), rws, W_O + (br * 128 + pid) * 4608 + (32 * wave + 8 * (lane >> 4) + (lane & 7)) * 16, 0, SC1);
  }
}

extern "C" void kernel_launch(void* const* d_in, const int* in_sizes, int n_in, void* d_out, int out_size, void* d_ws, size_t ws_size, hipStream_t stream) {
  Params p;
  p.xa = (const unsigned short*)d_in[0]; p.ka = (const unsigned short*)d_in[1]; p.va = (const unsigned short*)d_in[2]; p.wqa = (const unsigned short*)d_in[3]; p.woa = (const unsigned short*)d_in[4];
  p.xb = (const unsigned short*)d_in[5]; p.kb = (const unsigned short*)d_in[6]; p.vb = (const unsigned short*)d_in[7]; p.wqb = (const unsigned short*)d_in[8]; p.wob = (const unsigned short*)d_in[9];
  p.out = (unsigned short*)d_out;
  p.ws = (unsigned char*)d_ws + WSOFF;
  (void)hipFuncSetAttribute((const void*)attn_fused, hipFuncAttributeMaxDynamicSharedMemorySize, L_TOTAL);
  attn_fused<<<256, 640, L_TOTAL, stream>>>(p);
}
```

```cpp
#include <hip/hip_runtime.h>
#include <stdint.h>

typedef unsigned u32x4 __attribute__((ext_vector_type(4)));
typedef unsigned u32x2 __attribute__((ext_vector_type(2)));
typedef float f32x4 __attribute__((ext_vector_type(4)));
typedef float f32x2 __attribute__((ext_vector_type(2)));
typedef __bf16 bf16x2 __attribute__((ext_vector_type(2)));
#define LAS __attribute__((address_space(3)))

#define TAU0 14300
#define TAU1 14170
#define TAU2 14170
#define TAU3 14270
#define DQ8 4
#define ANCH 30000
#define WIN 48
#define WP 20
#define WT 12
#define WU 28
#define MIDSLEEP 6
#define QDELAY 40
#define H1DELAY 36
#define H2DELAY 30

constexpr int DM = 576;
constexpr int RINGB = 36 * 4096;
constexpr int UQ = 9;
constexpr int UKV = 256;
constexpr int UWO = UQ + UKV;
constexpr int L_WQ = 27 * 4096, L_WO = (UKV % 36) * 4096;
static_assert(UKV % 36 + 9 <= 36, "w_o image must not wrap");
static_assert(WIN % 4 == 0 && WIN <= 60 && WIN >= 36 && WT % 4 == 0 && WT <= 36 && WP % 4 == 0, "vmcnt is 6 bits; units are 4 loads");
constexpr int L_CTRL = RINGB;
constexpr int C_LANDED = 0, C_QCNT = 1, C_QREADY = 2, C_TAG = 3, C_KCNT = 4, C_LCNT = 6, C_PROG = 8, C_RCNT = 16, C_PUB = 18, C_DUE = 20, C_ADJ = 21;
constexpr int L_STAT = L_CTRL + 256;
constexpr int L_QBUF = L_STAT + 256;
constexpr int L_MRG = L_QBUF + 1024;
constexpr int L_QPART = L_MRG;
constexpr int L_OBUF = L_MRG + 8192;
constexpr int L_TOTAL = L_OBUF + 128;
constexpr int W_EPOCH = 0;
constexpr int W_LSE = 4096;
constexpr int W_QA = 24576;
constexpr int W_Q = 32768;
constexpr int W_P = W_Q + 16 * 16384;
constexpr int W_O = W_P + 16 * 65536;
constexpr int W_END = W_O + 2 * 128 * 4608;
constexpr int SPIN_CAP = 1 << 19;
constexpr int GSPIN_CAP = 1 << 16;

struct Params {
  const unsigned short *ka, *va, *kb, *vb, *wqa, *wqb, *woa, *wob, *xa, *xb;
  unsigned short* out;
  unsigned char* ws;
};

__device__ __forceinline__ float dot2(unsigned k, unsigned q, float acc) {
  return __builtin_amdgcn_fdot2_f32_bf16(__builtin_bit_cast(bf16x2, k), __builtin_bit_cast(bf16x2, q), acc, false);
}
template <int CTRL> __device__ __forceinline__ float dpp(float x) {
  return __builtin_bit_cast(float, __builtin_amdgcn_mov_dpp(__builtin_bit_cast(int, x), CTRL, 0xf, 0xf, true));
}
__device__ __forceinline__ float bflo(unsigned u) { return __builtin_bit_cast(float, u << 16); }
__device__ __forceinline__ float bfhi(unsigned u) { return __builtin_bit_cast(float, u & 0xffff0000u); }
__device__ __forceinline__ unsigned pack_bf16(float lo, float hi) {
  bf16x2 v = {(__bf16)lo, (__bf16)hi};
  return __builtin_bit_cast(unsigned, v);
}
__device__ __forceinline__ float fu(unsigned u) { return __builtin_bit_cast(float, u); }
__device__ __forceinline__ unsigned uf(float f) { return __builtin_bit_cast(unsigned, f); }
constexpr int XOR1 = 0xB1, XOR2 = 0x4E, XOR7 = 0x141, XOR8 = 0x128, ROR4 = 0x124, BCAST = 0x150;
__device__ __forceinline__ float swap32_add(float a, float b) {
  auto s = __builtin_amdgcn_permlane32_swap(uf(a), uf(b), false, false);
  return fu(s[0]) + fu(s[1]);
}
__device__ __forceinline__ float swap16_add(float a, float b) {
  auto s = __builtin_amdgcn_permlane16_swap(uf(a), uf(b), false, false);
  return fu(s[0]) + fu(s[1]);
}
__device__ __forceinline__ float xrow_max(float x) {
  auto s = __builtin_amdgcn_permlane16_swap(uf(x), uf(x), false, false);
  x = fmaxf(fu(s[0]), fu(s[1]));
  auto t = __builtin_amdgcn_permlane32_swap(uf(x), uf(x), false, false);
  return fmaxf(fu(t[0]), fu(t[1]));
}
#define BARRIER() asm volatile("s_waitcnt lgkmcnt(0)\n\ts_barrier" ::: "memory")
#define MEMBAR() asm volatile("" ::: "memory")
#define LGKM0() asm volatile("s_waitcnt lgkmcnt(0)" ::: "memory")
constexpr int SC1 = 16;
constexpr unsigned TAGX = 0xA5C3965Au;
__device__ __forceinline__ u32x4 pair(float a, float b, unsigned tag) { return u32x4{uf(a), tag, uf(b), tag ^ TAGX}; }

__device__ __forceinline__ void wait_gt(volatile LAS unsigned* w, unsigned x) {
#pragma nounroll
  for (int sp = 0; sp < SPIN_CAP; ++sp) {
    if (__builtin_amdgcn_readfirstlane(*w) > x) break;
    __builtin_amdgcn_s_sleep(1);
  }
  MEMBAR();
}
template <int N, int PAUSE>
__device__ __forceinline__ void gather(const __amdgpu_buffer_rsrc_t rws, int off, int stride, unsigned tag, u32x4 (&v)[N]) {
  bool ok = false;
#pragma nounroll
  for (int sp = 0; sp < GSPIN_CAP; ++sp) {
    if (!ok) {
#pragma unroll
      for (int k = 0; k < N; ++k) v[k] = __builtin_amdgcn_raw_buffer_load_b128(rws, off + k * stride, 0, SC1);
      ok = true;
#pragma unroll
      for (int k = 0; k < N; ++k) ok &= (v[k][1] == tag) & (v[k][3] == (tag ^ TAGX));
    }
    if (__all(ok)) break;
    if (PAUSE) __builtin_amdgcn_s_sleep(PAUSE);
    MEMBAR();
  }
}

#define UNITW(w, va, vb, vc, vd) "s_waitcnt vmcnt(%[" w "])\n\tds_max_i32 %[vpa], %[vpub]\n\t" \
      "buffer_load_dwordx4 %[" va "], s[40:43], %[sso] offen nt lds\n\t" \
      "buffer_load_dwordx4 %[" vb "], s[40:43], %[sso] offen offset:1024 nt lds\n\t" \
      "buffer_load_dwordx4 %[" vc "], s[40:43], %[sso] offen offset:2048 nt lds\n\t" \
      "buffer_load_dwordx4 %[" vd "], s[40:43], %[sso] offen offset:3072 nt lds\n\t" \
      "s_add_u32 m0, m0, 4096\n\tv_add_u32 %[vpub], 1, %[vpub]\n\t"
#define UNIT(v) UNITW("WM4", v, v, v, v)
#define ROT(v) "v_add_u32 v60, 0x4000, %[" v "]\n\tv_bfi_b32 %[" v "], s23, v60, %[" v "]\n\t"

extern "C" __global__ void __launch_bounds__(640, 3) attn_fused(Params p) {
  extern __shared__ __attribute__((aligned(16))) unsigned char smem[];
  LAS unsigned char* L = (LAS unsigned char*)smem;
  const int tid = threadIdx.x, wave = __builtin_amdgcn_readfirstlane(tid >> 6);
  const int bid = blockIdx.x;
  const int wi16 = bid >> 4, kvh = bid & 15;
  const int br = wi16 >> 3, pid = kvh * 8 + (wi16 & 7);
  const int qb = (bid >> 2) & 1, rq = bid & 3, qh = bid >> 3;
  {
    const unsigned ldsbase = (unsigned)(uintptr_t)L;
    unsigned v0, v1, v2, v3, vctrl, vprog, vpub, vpa;
    unsigned sn, sslot, sso, sdue, st1, sspin;
    asm volatile(
      "s_load_dwordx16 s[24:39], %[karg], 0x0\n\t"
      "s_memrealtime s[20:21]\n\t"
      "v_mov_b32 v62, 0\n\t"
      "v_mov_b32 %[vctrl], %[ctrl]\n\t"
      "v_lshl_add_u32 v59, %[tid], 2, %[vctrl]\n\t"
      "s_cmp_eq_u32 %[wave], 0\n\t"
      "s_cbranch_scc1 .Lload%=\n\t"
      "s_barrier\n\t"
      "s_waitcnt lgkmcnt(0)\n\t"
      "s_branch .Lend%=\n\t"
      ".Lload%=:\n\t"
      "ds_write_b32 v59, v62\n\t"
      "s_setprio 3\n\t"
      "v_lshrrev_b32 v60, 4, %[tid]\n\t"
      "v_and_b32 v61, 15, %[tid]\n\t"
      "v_lshlrev_b32 v60, 13, v60\n\t"
      "v_lshl_or_b32 %[v0], v61, 4, v60\n\t"
      "v_add_u32 %[v1], 0x7c00, %[v0]\n\t"
      "v_add_u32 %[v2], 0xf800, %[v0]\n\t"
      "v_add_u32 %[v3], 0x17400, %[v0]\n\t"
      "s_mov_b32 %[sso], %[qoff]\n\t"
      "v_and_b32 v61, 3, %[tid]\n\t"
      "s_mov_b32 s42, 0x480000\n\t"
      "s_mov_b32 s43, 0x20000\n\t"
      "s_add_u32 m0, %[ldsbase], %[WQo]\n\t"
      "s_mov_b32 %[sn], 0\n\t"
      "v_mov_b32 %[vpub], %[PUB0]\n\t"
      "v_lshl_add_u32 %[vpa], %[tid], 20, %[vctrl]\n\t"
      "v_lshl_add_u32 %[vprog], v61, 2, %[vctrl]\n\t"
      "s_cmp_eq_u32 %[qb], 0\n\t"
      "s_waitcnt lgkmcnt(0)\n\t"
      "s_cselect_b64 s[40:41], s[32:33], s[34:35]\n\t"
      ".Lwq%=:\n\t"
      UNITW("WM4", "v0", "v1", "v2", "v3")
      "s_add_u32 %[sso], %[sso], 0x20000\n\t"
      "s_add_u32 %[sn], %[sn], 1\n\t"
      "s_cmp_lt_u32 %[sn], 9\n\t"
      "s_cbranch_scc1 .Lwq%=\n\t"
      "s_barrier\n\t"
      "s_lshl_b32 %[sdue], s20, 8\n\t"
      "s_add_u32 %[sdue], %[sdue], %[ANCc]\n\t"
      "s_lshl_b32 %[st1], %[wi16], 12\n\t"
      "v_lshl_add_u32 %[v0], %[tid], 4, %[st1]\n\t"
      "s_mov_b32 s23, 0xffff\n\t"
      "v_add_u32 v60, 0x5000, %[v0]\n\t"
      "v_add_u32 v61, 0xa000, %[v0]\n\t"
      "v_add_u32 v62, 0xf000, %[v0]\n\t"
      "v_and_b32 v60, s23, v60\n\t"
      "v_and_b32 v61, s23, v61\n\t"
      "v_and_b32 v62, s23, v62\n\t"
      "v_or_b32 %[v1], 0x10000, v60\n\t"
      "v_or_b32 %[v2], 0x20000, v61\n\t"
      "v_or_b32 %[v3], 0x30000, v62\n\t"
      "s_cmp_lg_u32 %[br], 0\n\t"
      "s_cselect_b64 s[36:37], s[38:39], s[36:37]\n\t"
      "s_mov_b32 s42, 0x4000000\n\t"
      "s_mov_b32 %[sn], 0\n\t"
      "s_mov_b32 %[sslot], %[ldsbase]\n\t"
      "s_mov_b32 s19, 0\n\t"
      "s_mov_b32 s16, %[T01]\n\t"
      "s_mov_b32 s17, %[T23]\n\t"
      "s_waitcnt vmcnt(20)\n\t"
      "s_movk_i32 %[sspin], 100\n\t"
      ".Lp0%=:\n\t"
      "s_memrealtime s[20:21]\n\t"
      "s_sub_u32 %[sspin], %[sspin], 1\n\t"
      "s_waitcnt lgkmcnt(0)\n\t"
      "s_cbranch_scc1 .Ltensor%=\n\t"
      "s_lshl_b32 s22, s20, 8\n\t"
      "s_sub_u32 s22, s22, %[sdue]\n\t"
      "s_cmp_lt_i32 s22, 0\n\t"
      "s_cbranch_scc1 .Lp0%=\n\t"
      ".Ltensor%=:\n\t"
      "s_mov_b64 s[40:41], s[24:25]\n\t"
      "s_mov_b64 s[24:25], s[26:27]\n\t"
      "s_mov_b64 s[26:27], s[28:29]\n\t"
      "s_mov_b64 s[28:29], s[30:31]\n\t"
      "s_mov_b32 %[sso], %[kv0]\n\t"
      "s_and_b32 s18, s16, 0xffff\n\t"
      "s_lshr_b64 s[16:17], s[16:17], 16\n\t"
      ".Lmain%=:\n\t"
      "s_mov_b32 m0, %[sslot]\n\t"
      "ds_read_b32 v61, %[vprog] offset:32\n\t"
      "ds_read_b32 v62, %[vctrl] offset:84\n\t"
      UNIT("v0") UNIT("v1")
      "s_cmp_eq_u32 s19, 0\n\t"
      "s_cbranch_scc1 .Lnomid%=\n\t"
      "s_sleep %c[MS]\n\t"
      "s_mov_b32 s19, 0\n\t"
      ".Lnomid%=:\n\t"
      UNIT("v2") UNIT("v3")
      "s_memrealtime s[20:21]\n\t"
      ROT("v0") ROT("v1") ROT("v2") ROT("v3")
      "s_add_u32 %[sso], %[sso], 0x40000\n\t"
      "s_and_b32 %[sso], %[sso], 0x3fffff\n\t"
      "s_or_b32 %[sso], %[sso], %[kvb]\n\t"
      "s_add_u32 %[sslot], %[sslot], 16384\n\t"
      "s_cmp_lt_u32 %[sslot], %[ctrl]\n\t"
      "s_cselect_b32 %[sslot], %[sslot], %[ldsbase]\n\t"
      "s_sub_u32 %[st1], %[sn], 8\n\t"
      "s_ashr_i32 %[st1], %[st1], 1\n\t"
      "s_add_u32 %[sn], %[sn], 1\n\t"
      "s_add_u32 %[sdue], %[sdue], s18\n\t"
      "s_cmp_eq_u32 %[sn], 1\n\t"
      "s_cbranch_scc1 .Lwqpub%=\n\t"
      ".Lsched%=:\n\t"
      "s_waitcnt lgkmcnt(0)\n\t"
      "v_readfirstlane_b32 s15, v62\n\t"
      "v_cmp_ge_i32 vcc, %[st1], v61\n\t"
      "s_add_u32 s15, s15, %[sdue]\n\t"
      "s_lshl_b32 s22, s20, 8\n\t"
      "s_sub_u32 s22, s22, s15\n\t"
      "s_cmp_lt_i32 s22, 0\n\t"
      "s_cbranch_scc1 .Lpace%=\n\t"
      ".Lpaced%=:\n\t"
      "s_cbranch_vccnz .Lfull%=\n\t"
      ".Lfree%=:\n\t"
      "v_xor_b32 %[vprog], 16, %[vprog]\n\t"
      "s_cmp_eq_u32 %[sn], 6\n\t"
      "s_cbranch_scc1 .Lqchk%=\n\t"
      "s_and_b32 %[st1], %[sn], 15\n\t"
      "s_cbranch_scc1 .Lmain%=\n\t"
      "s_cmp_lt_u32 %[sn], 64\n\t"
      "s_cbranch_scc1 .Ltensor%=\n\t"
      "v_and_b32 v60, 7, %[tid]\n\t"
      "v_lshl_add_u32 %[vprog], v60, 2, %[vctrl]\n\t"
      "s_mov_b32 %[sspin], %[CAP]\n\t"
      ".Lwochk%=:\n\t"
      "ds_read_b32 v61, %[vprog] offset:32\n\t"
      "s_sub_u32 %[sspin], %[sspin], 1\n\t"
      "s_waitcnt lgkmcnt(0)\n\t"
      "s_cbranch_scc1 .Ldrain%=\n\t"
      "v_cmp_ge_i32 vcc, 28, v61\n\t"
      "s_cbranch_vccz .Lwogo%=\n\t"
      "v_mov_b32 v58, %[UWOc]\n\t"
      "s_waitcnt vmcnt(0)\n\t"
      "ds_max_i32 %[vpa], v58\n\t"
      "s_branch .Lwochk%=\n\t"
      ".Lwogo%=:\n\t"
      "s_mul_i32 %[sso], %[pid], 0x9000\n\t"
      "s_mov_b32 s42, 0x480000\n\t"
      "s_mov_b64 s[40:41], s[36:37]\n\t"
      "v_lshlrev_b32 %[v0], 4, %[tid]\n\t"
      "s_mov_b32 m0, %[sslot]\n\t"
      "s_mov_b32 %[sn], 0\n\t"
      "v_mov_b32 v58, %c[UWOc]-7\n\t" "v_mov_b32 v59, %c[UWOc]-6\n\t" "v_mov_b32 v60, %c[UWOc]-5\n\t" "v_mov_b32 v62, %c[UWOc]-4\n\t" "v_mov_b32 v61, %c[UWOc]-3\n\t"
      ".if %c[WT4] < 28\n\ts_waitcnt vmcnt(28)\n\tds_max_i32 %[vpa], v58\n\t.endif\n\t"
      ".if %c[WT4] < 24\n\ts_waitcnt vmcnt(24)\n\tds_max_i32 %[vpa], v59\n\t.endif\n\t"
      ".if %c[WT4] < 20\n\ts_waitcnt vmcnt(20)\n\tds_max_i32 %[vpa], v60\n\t.endif\n\t"
      ".if %c[WT4] < 16\n\ts_waitcnt vmcnt(16)\n\tds_max_i32 %[vpa], v62\n\t.endif\n\t"
      ".if %c[WT4] < 12\n\ts_waitcnt vmcnt(12)\n\tds_max_i32 %[vpa], v61\n\t.endif\n\t"
      ".if %c[WT4] < 8\n\tv_mov_b32 v58, %c[UWOc]-2\n\ts_waitcnt vmcnt(8)\n\tds_max_i32 %[vpa], v58\n\t.endif\n\t"
      ".if %c[WT4] < 4\n\tv_mov_b32 v59, %c[UWOc]-1\n\ts_waitcnt vmcnt(4)\n\tds_max_i32 %[vpa], v59\n\t.endif\n\t"
      "v_add_u32 %[vpub], %[DWT], %[vpub]\n\t"
      ".Lwo%=:\n\t"
      UNITW("WT4", "v0", "v0", "v0", "v0")
      "s_add_u32 %[sso], %[sso], 0x1000\n\t"
      "s_add_u32 %[sn], %[sn], 1\n\t"
      "s_cmp_lt_u32 %[sn], %[NWT]\n\t"
      "s_cbranch_scc1 .Lwo%=\n\t"
      "v_mov_b32 %[vpub], 0\n\t"
      ".Lwo2%=:\n\t"
      "s_cmp_ge_u32 %[sn], 9\n\t"
      "s_cbranch_scc1 .Ldrain%=\n\t"
      UNITW("WU4", "v0", "v0", "v0", "v0")
      "s_add_u32 %[sso], %[sso], 0x1000\n\t"
      "s_add_u32 %[sn], %[sn], 1\n\t"
      "s_branch .Lwo2%=\n\t"
      ".Lwqpub%=:\n\t"
      "v_mov_b32 v58, 9\n\t"
      "s_waitcnt vmcnt(16) lgkmcnt(0)\n\t"
      "s_memrealtime s[20:21]\n\t"
      "s_waitcnt lgkmcnt(0)\n\t"
      "s_lshl_b32 s22, s20, 8\n\t"
      "s_sub_u32 %[sspin], s22, %[sdue]\n\t"
      "s_cmp_gt_i32 %[sspin], 0\n\t"
      "s_cselect_b32 %[sdue], s22, %[sdue]\n\t"
      "v_mov_b32 v59, %[sdue]\n\t"
      "ds_write_b32 %[vpa], v59 offset:80\n\t"
      "ds_max_i32 %[vpa], v58\n\t"
      "s_branch .Lsched%=\n\t"
      ".Lpace%=:\n\t"
      "s_cmp_ge_u32 %[sn], 64\n\t"
      "s_cbranch_scc1 .Lpaced%=\n\t"
      "s_movk_i32 %[sspin], 100\n\t"
      ".Lpace1%=:\n\t"
      "s_memrealtime s[20:21]\n\t"
      "s_sub_u32 %[sspin], %[sspin], 1\n\t"
      "s_waitcnt lgkmcnt(0)\n\t"
      "s_cbranch_scc1 .Lpaced%=\n\t"
      "s_lshl_b32 s22, s20, 8\n\t"
      "s_sub_u32 s22, s22, s15\n\t"
      "s_cmp_lt_i32 s22, 0\n\t"
      "s_cbranch_scc1 .Lpace1%=\n\t"
      "s_mov_b32 s19, 1\n\t"
      "v_add_u32 v58, %[LAGP], %[vpub]\n\t"
      "s_waitcnt vmcnt(%[WPc])\n\t"
      "ds_max_i32 %[vpa], v58\n\t"
      "s_branch .Lpaced%=\n\t"
      ".Lfull%=:\n\t"
      "ds_read_b32 v61, %[vprog] offset:32\n\t"
      "s_waitcnt lgkmcnt(0)\n\t"
      "v_cmp_ge_i32 vcc, %[st1], v61\n\t"
      "s_cbranch_vccz .Lfree%=\n\t"
      "v_add_u32 v59, %[LAGc], %[vpub]\n\t"
      "s_waitcnt vmcnt(0)\n\t"
      "ds_max_i32 %[vpa], v59\n\t"
      "s_mov_b32 %[sspin], %[CAP]\n\t"
      ".Lspin%=:\n\t"
      "ds_read_b32 v61, %[vprog] offset:32\n\t"
      "s_sub_u32 %[sspin], %[sspin], 1\n\t"
      "s_waitcnt lgkmcnt(0)\n\t"
      "s_cbranch_scc1 .Ldrain%=\n\t"
      "v_cmp_ge_i32 vcc, %[st1], v61\n\t"
      "s_cbranch_vccnz .Lspin%=\n\t"
      "s_branch .Lfree%=\n\t"
      ".Lqchk%=:\n\t"
      "s_mov_b32 %[sspin], %[CAP]\n\t"
      ".Lqchk1%=:\n\t"
      "ds_read_b32 v61, %[vctrl] offset:4\n\t"
      "s_sub_u32 %[sspin], %[sspin], 1\n\t"
      "s_waitcnt lgkmcnt(0)\n\t"
      "s_cbranch_scc1 .Ldrain%=\n\t"
      "v_cmp_ge_i32 vcc, 7, v61\n\t"
      "s_cbranch_vccnz .Lqchk1%=\n\t"
      "s_branch .Lmain%=\n\t"
      ".Ldrain%=:\n\t"
      "v_mov_b32 v62, 0x40000000\n\t"
      "s_waitcnt vmcnt(0)\n\t"
      "ds_write_b32 %[vpa], v62\n\t"
      "s_setprio 0\n\t"
      "s_waitcnt lgkmcnt(0)\n\t"
      ".Lend%=:\n\t"
      : [v0] "=&v"(v0), [v1] "=&v"(v1), [v2] "=&v"(v2), [v3] "=&v"(v3), [vctrl] "=&v"(vctrl), [vprog] "=&v"(vprog), [vpub] "=&v"(vpub), [vpa] "=&v"(vpa),
        [sn] "=&s"(sn), [sslot] "=&s"(sslot), [sso] "=&s"(sso), [sdue] "=&s"(sdue), [st1] "=&s"(st1), [sspin] "=&s"(sspin)
      : [tid] "v"(tid), [wave] "s"(wave), [karg] "s"(__builtin_amdgcn_kernarg_segment_ptr()), [kvb] "s"(kvh << 22), [kv0] "s"((kvh << 22) | ((((kvh >> 3) * DQ8) & 15) << 18)), [pid] "s"(pid), [br] "s"(br), [wi16] "s"(wi16),
        [qb] "s"(qb), [qoff] "s"(rq * (144 * 8192) + qh * 256), [ldsbase] "s"(ldsbase), [ctrl] "s"(ldsbase + L_CTRL),
        [WM4] "n"(WIN - 4), [WT4] "n"(WT - 4), [WU4] "n"(WU - 4), [NWT] "n"(WT / 4), [DWT] "n"((WIN - WT) / 4), [PUB0] "n"(1 - WIN / 4), [LAGc] "n"(WIN / 4 - 1), [LAGP] "n"(WIN / 4 - 1 - WP / 4),
        [WPc] "n"(WP), [UWOc] "n"(UWO), [WQo] "n"(L_WQ), [MS] "n"(MIDSLEEP), [CAP] "n"(SPIN_CAP), [T01] "n"(TAU0 | (TAU1 << 16)), [T23] "n"(TAU2 | (TAU3 << 16)), [ANCc] "n"(ANCH)
      : "memory", "scc", "vcc", "m0", "s15", "s16", "s17", "s18", "s19", "s20", "s21", "s22", "s23", "s24", "s25", "s26", "s27", "s28", "s29", "s30", "s31", "s32", "s33", "s34", "s35",
        "s36", "s37", "s38", "s39", "s40", "s41", "s42", "s43", "v58", "v59", "v60", "v61", "v62");
  }
  const int lane = tid & 63;
  volatile LAS unsigned* ctrl = (volatile LAS unsigned*)(L + L_CTRL);
  LAS unsigned* actrl = (LAS unsigned*)(L + L_CTRL);
  const __amdgpu_buffer_rsrc_t rws = __builtin_amdgcn_make_buffer_rsrc((void*)p.ws, 0, W_END, 0x00020000);

  if (wave == 9) {
    unsigned tag;
    asm volatile("s_load_dword %0, %1, 0x0\n\ts_waitcnt lgkmcnt(0)" : "=&s"(tag) : "s"(p.ws + W_EPOCH) : "memory");
    tag += 1u;
    if (lane == 0) ctrl[C_TAG] = tag;
    {
      wait_gt(ctrl + C_QCNT, 7);
      f32x2 s2 = {0.f, 0.f};
#pragma unroll
      for (int w8 = 0; w8 < 8; ++w8) s2 += *(LAS f32x2*)(L + L_QPART + w8 * 512 + lane * 8);
      __builtin_amdgcn_raw_buffer_store_b128(pair(s2[0], s2[1], tag), rws, W_Q + (qh >> 1) * 16384 + qb * 8192 + rq * 2048 + (qh & 1) * 1024 + lane * 16, 0, SC1);
      const unsigned ownA = __builtin_amdgcn_readfirstlane(ctrl[C_DUE]);
      if (lane == 0) __builtin_amdgcn_raw_buffer_store_b128(pair(fu(ownA), fu(ownA), tag), rws, W_QA + bid * 16, 0, SC1);
      __builtin_amdgcn_s_sleep(QDELAY);
#pragma nounroll
      for (int b = 0; b < 2; ++b) {
        u32x4 v[8];
        gather<8, 10>(rws, W_Q + kvh * 16384 + b * 8192 + lane * 16, 1024, tag, v);
#pragma unroll
        for (int h = 0; h < 2; ++h)
          ((LAS unsigned*)(L + L_QBUF))[b * 128 + h * 64 + lane] = pack_bf16((fu(v[h][0]) + fu(v[h + 2][0])) + (fu(v[h + 4][0]) + fu(v[h + 6][0])),
                                                                             (fu(v[h][2]) + fu(v[h + 2][2])) + (fu(v[h + 4][2]) + fu(v[h + 6][2])));
        LGKM0();
        if (lane == 0) ctrl[C_QREADY] = b + 1;
        if (b == 1) {
          const u32x4 va = __builtin_amdgcn_raw_buffer_load_b128(rws, W_QA + kvh * 256 + (lane & 15) * 16, 0, SC1);
          const int dd = (int)(va[0] - ownA);
          const bool g = va[1] == tag && va[3] == (tag ^ TAGX) && dd > -200000 && dd < 200000;
          float d = g ? (float)dd : 0.f, n = g ? 1.f : 0.f;
          d += dpp<XOR1>(d); d += dpp<XOR2>(d); d += dpp<XOR7>(d); d += dpp<XOR8>(d);
          n += dpp<XOR1>(n); n += dpp<XOR2>(n); n += dpp<XOR7>(n); n += dpp<XOR8>(n);
          if (lane == 0 && n > 7.f) ctrl[C_ADJ] = (unsigned)(int)(d / n);
        }
      }
    }
    {
      wait_gt(ctrl + C_LANDED, UWO - 24);
      u32x4 vl[4];
      gather<4, 0>(rws, W_LSE + kvh * 1024 + (wi16 >> 2) * 256 + (lane >> 4) * 16, 64, tag, vl);
      const float MM = xrow_max(fmaxf(fmaxf(fu(vl[0][0]), fu(vl[1][0])), fmaxf(fu(vl[2][0]), fu(vl[3][0]))));
      float ww[4], tl = 0.f;
#pragma unroll
      for (int k = 0; k < 4; ++k) { ww[k] = __builtin_amdgcn_exp2f(fu(vl[k][0]) - MM); tl = fmaf(ww[k], fu(vl[k][2]), tl); }
      tl = swap16_add(tl, tl); tl = swap32_add(tl, tl);
#pragma unroll
      for (int k = 0; k < 4; ++k) ww[k] = ww[k] / tl;
      LAS float* odst = (LAS float*)(L + L_OBUF) + (2 * ((lane >> 2) & 3) + (lane >> 5)) * 4 + (lane & 3);
      int poff = W_P + kvh * 65536 + wi16 * 4096 + lane * 16;
      asm volatile("" : "+v"(ww[0]), "+v"(ww[1]), "+v"(ww[2]), "+v"(ww[3]), "+v"(odst), "+v"(poff));
#pragma nounroll
      for (int sp = 0; ctrl[C_PUB + br] == 0u && sp < SPIN_CAP; ++sp) {}
      MEMBAR();
      __builtin_amdgcn_s_sleep(H1DELAY);
      u32x4 v[4];
      gather<4, 0>(rws, poff, 1024, tag, v);
      const float t0 = fmaf(ww[0], fu(v[0][0]), ww[1] * fu(v[1][0])) + fmaf(ww[2], fu(v[2][0]), ww[3] * fu(v[3][0]));
      const float t1 = fmaf(ww[0], fu(v[0][2]), ww[1] * fu(v[1][2])) + fmaf(ww[2], fu(v[2][2]), ww[3] * fu(v[3][2]));
      float ov = swap32_add(t0, t1);
      ov = swap16_add(ov, ov);
      *odst = bflo(pack_bf16(ov, ov));
    }
    BARRIER();
    if (pid < 72) {
      __builtin_amdgcn_s_sleep(H2DELAY);
      u32x4 v[8];
      gather<8, 0>(rws, W_O + (br * 128 + 8 * (lane >> 2)) * 4608 + (pid * 4 + (lane & 3)) * 16, 4608, tag, v);
      asm volatile(".Lattn_tail:");
      float s0 = 0.f, s1 = 0.f;
#pragma unroll
      for (int k = 0; k < 8; ++k) { s0 += fu(v[k][0]); s1 += fu(v[k][2]); }
      s0 += dpp<ROR4>(s0); s1 += dpp<ROR4>(s1);
      s0 += dpp<XOR8>(s0); s1 += dpp<XOR8>(s1);
      s0 = swap16_add(s0, s0); s1 = swap16_add(s1, s1);
      s0 = swap32_add(s0, s0); s1 = swap32_add(s1, s1);
      if (lane < 4) __builtin_nontemporal_store(pack_bf16(s0, s1), (unsigned*)p.out + br * 288 + (pid >> 3) * 32 + ((pid & 1) * 4 + lane) * 4 + ((pid >> 1) & 3));
      if (bid == 0 && lane == 0) __builtin_amdgcn_raw_buffer_store_b32(tag, rws, W_EPOCH, 0, SC1);
    }
    return;
  }

  if (wave != 0) {
    const int wi = wave - 1;
    u32x4 pf, pf2;
    {
      unsigned long long pc;
      unsigned so, sz;
      asm volatile("s_getpc_b64 %0\n\t.Lpc%=:\n\ts_mov_b32 %1, .Lpc%=-attn_fused\n\ts_mov_b32 %2, .Lattn_tail-attn_fused" : "=s"(pc), "=s"(so), "=s"(sz));
      const __amdgpu_buffer_rsrc_t rc = __builtin_amdgcn_make_buffer_rsrc((void*)(pc - so), 0, sz, 0x00020000);
      pf = __builtin_amdgcn_raw_buffer_load_b128(rc, wave * 1024 + lane * 16, 0, 0);
      pf2 = __builtin_amdgcn_raw_buffer_load_b128(rc, 8192 + wave * 1024 + lane * 16, 0, 0);
    }
    {
      const __amdgpu_buffer_rsrc_t rx = __builtin_amdgcn_make_buffer_rsrc((void*)((qb ? p.xb : p.xa) + rq * 144), 0, 144 * 2, 0x00020000);
      unsigned short xv[5];
#pragma unroll
      for (int k = 0; k < 5; ++k) xv[k] = __builtin_amdgcn_raw_buffer_load_b16(rx, (4 * wi + (lane >> 4) + 32 * k) * 2, 0, 0);
      __builtin_amdgcn_sched_barrier(0);
      wait_gt(ctrl + C_LANDED, UQ - 1);
      float a[8];
#pragma unroll
      for (int e = 0; e < 8; ++e) a[e] = 0.f;
#pragma unroll
      for (int k = 0; k < 5; ++k) {
        if (k == 4 && wi >= 4) break;
        const u32x4 w = *(LAS u32x4*)(L + L_WQ + k * 8192 + wi * 1024 + lane * 16);
        const float xf = bflo(xv[k]);
#pragma unroll
        for (int e = 0; e < 4; ++e) { a[2 * e] = fmaf(xf, bflo(w[e]), a[2 * e]); a[2 * e + 1] = fmaf(xf, bfhi(w[e]), a[2 * e + 1]); }
      }
      asm volatile("" :: "v"(pf), "v"(pf2));
      float u4[4], u2[2];
#pragma unroll
      for (int e = 0; e < 4; ++e) u4[e] = swap32_add(a[e], a[e + 4]);
#pragma unroll
      for (int e = 0; e < 2; ++e) u2[e] = swap16_add(u4[e], u4[e + 2]);
      *(LAS f32x2*)(L + L_QPART + wi * 512 + (lane & 15) * 32 + (lane >> 4) * 8) = f32x2{u2[0], u2[1]};
      LGKM0();
      if (lane == 0) __hip_atomic_fetch_add(actrl + C_QCNT, 1u, __ATOMIC_RELAXED, __HIP_MEMORY_SCOPE_WORKGROUP);
    }
    const int r = lane >> 4, c = lane & 15;
    constexpr float C2 = 0.088388347648318440f * 1.4426950408889634f;
    unsigned u = UQ + wi;
    int uoff = wi * 4096;
    unsigned done = 0;
#define NEXT_UNIT(R) wait_gt(ctrl + C_LANDED, u); \
      { const LAS u32x4* sp4 = (const LAS u32x4*)(L + uoff) + lane; \
        _Pragma("unroll") for (int t = 0; t < 4; ++t) R[t] = sp4[t * 64]; } \
      asm volatile("s_waitcnt lgkmcnt(0)" : "+v"(R[0]), "+v"(R[1]), "+v"(R[2]), "+v"(R[3])::"memory"); \
      ++done; if (lane == 0) ctrl[C_PROG + wi] = done; \
      __builtin_amdgcn_sched_barrier(0); \
      u += 8; uoff += 8 * 4096; if (uoff >= RINGB) uoff -= RINGB;
#pragma nounroll
    for (int b = 0; b < 2; ++b) {
      u32x4 q0, q1;
      float s0 = 0.f, s1 = 0.f, s2 = 0.f, s3 = 0.f;
#pragma nounroll
      for (int it = 0; it < 4; ++it) {
        u32x4 Ka[4], Kb[4];
        NEXT_UNIT(Ka)
        NEXT_UNIT(Kb)
        if (it == 0) {
          wait_gt(ctrl + C_QREADY, b);
          q0 = *(LAS u32x4*)(L + L_QBUF + b * 512 + c * 16); q1 = *(LAS u32x4*)(L + L_QBUF + b * 512 + 256 + c * 16);
        }
        float x[16];
#pragma unroll
        for (int t = 0; t < 8; ++t) {
          float d0 = 0.f, d1 = 0.f;
#pragma unroll
          for (int k = 0; k < 4; ++k) { const unsigned kk = t < 4 ? Ka[t & 3][k] : Kb[t & 3][k]; d0 = dot2(kk, q0[k], d0); d1 = dot2(kk, q1[k], d1); }
          x[2 * t] = d0; x[2 * t + 1] = d1;
        }
#define TR_STEP(HALF, CTRL, BIT) _Pragma("unroll") for (int h = 0; h < (HALF); ++h) { \
          const float lo_ = x[h], hi_ = x[h + (HALF)]; const float t1_ = lo_ + dpp<CTRL>(lo_), t2_ = hi_ + dpp<CTRL>(hi_); \
          x[h] = (c & (BIT)) ? t2_ : t1_; }
        TR_STEP(8, XOR8, 8) TR_STEP(4, XOR7, 4) TR_STEP(2, XOR2, 2) TR_STEP(1, XOR1, 1)
#undef TR_STEP
        s0 = s1; s1 = s2; s2 = s3; s3 = x[0] * C2;
      }
      const unsigned tag = __builtin_amdgcn_readfirstlane(ctrl[C_TAG]);
      LAS float* st = (LAS float*)(L + L_STAT + b * 128);
      float mx = fmaxf(fmaxf(s0, s1), fmaxf(s2, s3));
      mx = fmaxf(mx, dpp<XOR2>(mx)); mx = fmaxf(mx, dpp<ROR4>(mx)); mx = fmaxf(mx, dpp<XOR8>(mx)); mx = xrow_max(mx);
      if (lane < 2) st[wi * 2 + lane] = mx;
      LGKM0();
      if (lane == 0) __hip_atomic_fetch_add(actrl + C_KCNT + b, 1u, __ATOMIC_RELAXED, __HIP_MEMORY_SCOPE_WORKGROUP);
      wait_gt(ctrl + C_KCNT + b, 7);
      mx = st[c];
      mx = fmaxf(mx, dpp<XOR2>(mx)); mx = fmaxf(mx, dpp<ROR4>(mx)); mx = fmaxf(mx, dpp<XOR8>(mx));
      s0 = __builtin_amdgcn_exp2f(s0 - mx); s1 = __builtin_amdgcn_exp2f(s1 - mx); s2 = __builtin_amdgcn_exp2f(s2 - mx); s3 = __builtin_amdgcn_exp2f(s3 - mx);
      float ls = (s0 + s1) + (s2 + s3);
      ls += dpp<XOR2>(ls); ls += dpp<ROR4>(ls); ls += dpp<XOR8>(ls); ls = swap16_add(ls, ls); ls = swap32_add(ls, ls);
      if (lane < 2) st[16 + wi * 2 + lane] = ls;
      LGKM0();
      unsigned arr = 0;
      if (lane == 0) arr = __hip_atomic_fetch_add(actrl + C_LCNT + b, 1u, __ATOMIC_RELAXED, __HIP_MEMORY_SCOPE_WORKGROUP);
      if (__builtin_amdgcn_readfirstlane(arr) == 7) {
        MEMBAR();
        float lt = st[16 + c];
        lt += dpp<XOR2>(lt); lt += dpp<ROR4>(lt); lt += dpp<XOR8>(lt);
        if (lane < 2) __builtin_amdgcn_raw_buffer_store_b128(pair(mx, lt, tag), rws, W_LSE + kvh * 1024 + (2 * b + lane) * 256 + wi16 * 16, 0, SC1);
      }
      float acc[2][8];
#pragma unroll
      for (int e = 0; e < 8; ++e) { acc[0][e] = 0.f; acc[1][e] = 0.f; }
#pragma nounroll
      for (int it = 0; it < 4; ++it) {
        const float pe = s0; s0 = s1; s1 = s2; s2 = s3;
#define PV(hf, t) { const float p0 = dpp<BCAST + 8 * hf + 2 * t>(pe), p1 = dpp<BCAST + 8 * hf + 2 * t + 1>(pe); \
          _Pragma("unroll") for (int k = 0; k < 4; ++k) { const float lo = bflo(V[t][k]), hi = bfhi(V[t][k]); \
            acc[0][2 * k] = fmaf(p0, lo, acc[0][2 * k]); acc[0][2 * k + 1] = fmaf(p0, hi, acc[0][2 * k + 1]); \
            acc[1][2 * k] = fmaf(p1, lo, acc[1][2 * k]); acc[1][2 * k + 1] = fmaf(p1, hi, acc[1][2 * k + 1]); } }
        { u32x4 V[4]; NEXT_UNIT(V) PV(0, 0) PV(0, 1) PV(0, 2) PV(0, 3) }
        asm volatile("" : "+v"(acc[0][0]), "+v"(acc[0][1]), "+v"(acc[0][2]), "+v"(acc[0][3]), "+v"(acc[0][4]), "+v"(acc[0][5]), "+v"(acc[0][6]), "+v"(acc[0][7]),
                          "+v"(acc[1][0]), "+v"(acc[1][1]), "+v"(acc[1][2]), "+v"(acc[1][3]), "+v"(acc[1][4]), "+v"(acc[1][5]), "+v"(acc[1][6]), "+v"(acc[1][7]));
        { u32x4 V[4];
          NEXT_UNIT(V) PV(1, 0) PV(1, 1) PV(1, 2) PV(1, 3) }
#undef PV
      }
      f32x4 o[7];
      unsigned rc = 0;
      if (wi == 7) {
        rc = ctrl[C_RCNT + b];
#pragma unroll
        for (int w8 = 0; w8 < 7; ++w8) o[w8] = *(volatile LAS f32x4*)(L + L_MRG + w8 * 1024 + lane * 16);
      }
      float y[8];
#pragma unroll
      for (int e = 0; e < 8; ++e) y[e] = swap32_add(acc[0][e], acc[1][e]);
      f32x4 z;
#pragma unroll
      for (int e = 0; e < 4; ++e) z[e] = swap16_add(y[e], y[e + 4]);
      if (wi != 7) {
        *(LAS f32x4*)(L + L_MRG + wi * 1024 + lane * 16) = z;
        LGKM0();
        if (lane == 0) __hip_atomic_fetch_add(actrl + C_RCNT + b, 1u, __ATOMIC_RELAXED, __HIP_MEMORY_SCOPE_WORKGROUP);
      } else {
        if (__builtin_amdgcn_readfirstlane(rc) < 7u) {
#pragma nounroll
          for (int sp = 0; ctrl[C_RCNT + b] < 7u && sp < SPIN_CAP; ++sp) {}
          MEMBAR();
#pragma unroll
          for (int w8 = 0; w8 < 7; ++w8) o[w8] = *(volatile LAS f32x4*)(L + L_MRG + w8 * 1024 + lane * 16);
        }
        z += ((o[0] + o[1]) + (o[2] + o[3])) + ((o[4] + o[5]) + o[6]);
        const int off = W_P + kvh * 65536 + (8 * b + 4 * (r >> 1) + (c >> 2)) * 4096 + wi16 * 256 + (r & 1) * 128 + (c & 3) * 16;
        __builtin_amdgcn_raw_buffer_store_b128(pair(z[0], z[1], tag), rws, off, 0, SC1);
        __builtin_amdgcn_raw_buffer_store_b128(pair(z[2], z[3], tag), rws, off + 64, 0, SC1);
        if (lane == 0) ctrl[C_PUB + b] = 1u;
      }
    }
  }
  {
    const unsigned tag = __builtin_amdgcn_readfirstlane(ctrl[C_TAG]);
    wait_gt(ctrl + C_LANDED, UWO + 9);
    float wf[4][8];
#pragma unroll
    for (int i = 0; i < 4; ++i) {
      const u32x4 wo = *(LAS u32x4*)(L + L_WO + ((lane >> 3) + 8 * i) * (DM * 2) + (8 * wave + (lane & 7)) * 16);
#pragma unroll
      for (int e = 0; e < 4; ++e) { wf[i][2 * e] = bflo(wo[e]); wf[i][2 * e + 1] = bfhi(wo[e]); }
    }
#pragma unroll
    for (int i = 0; i < 4; ++i)
      asm volatile("" : "+v"(wf[i][0]), "+v"(wf[i][1]), "+v"(wf[i][2]), "+v"(wf[i][3]), "+v"(wf[i][4]), "+v"(wf[i][5]), "+v"(wf[i][6]), "+v"(wf[i][7]));
    int ooff = (lane & 8) ? 0x7ff00000 : W_O + (br * 128 + pid) * 4608 + (32 * wave + 8 * (lane >> 4) + (lane & 7)) * 16;
    const LAS f32x4* osrc = (const LAS f32x4*)(L + L_OBUF + (lane >> 3) * 16);
    asm volatile("" : "+v"(ooff), "+v"(osrc));
    BARRIER();
    const f32x4 o4 = *osrc;
    float a[8];
#pragma unroll
    for (int e = 0; e < 8; ++e) a[e] = o4[0] * wf[0][e];
#pragma unroll
    for (int i = 1; i < 4; ++i)
#pragma unroll
      for (int e = 0; e < 8; ++e) a[e] = fmaf(o4[i], wf[i][e], a[e]);
    float u4[4], u2[2];
#pragma unroll
    for (int e = 0; e < 4; ++e) u4[e] = swap32_add(a[e], a[e + 4]);
#pragma unroll
    for (int e = 0; e < 2; ++e) u2[e] = swap16_add(u4[e], u4[e + 2]);
    const float t0 = u2[0] + dpp<XOR8>(u2[0]), t1 = u2[1] + dpp<XOR8>(u2[1]);
    __builtin_amdgcn_raw_buffer_store_b128(pair(t0, t1, tag), rws, ooff, 0, SC1);
  }
}

extern "C" void kernel_launch(void* const* d_in, const int* in_sizes, int n_in, void* d_out, int out_size, void* d_ws, size_t ws_size, hipStream_t stream) {
  Params p;
  p.xa = (const unsigned short*)d_in[0]; p.ka = (const unsigned short*)d_in[1]; p.va = (const unsigned short*)d_in[2]; p.wqa = (const unsigned short*)d_in[3]; p.woa = (const unsigned short*)d_in[4];
  p.xb = (const unsigned short*)d_in[5]; p.kb = (const unsigned short*)d_in[6]; p.vb = (const unsigned short*)d_in[7]; p.wqb = (const unsigned short*)d_in[8]; p.wob = (const unsigned short*)d_in[9];
  p.out = (unsigned short*)d_out;
  p.ws = (unsigned char*)d_ws;
  (void)hipFuncSetAttribute((const void*)attn_fused, hipFuncAttributeMaxDynamicSharedMemorySize, L_TOTAL);
  attn_fused<<<256, 640, L_TOTAL, stream>>>(p);
}
```
